```python
import torch

def _softmax(x, axis=-1):
    return torch.softmax(x, dim=axis)
def _gelu(x):
    return torch.nn.functional.gelu(x, approximate="tanh")
def _silu(x):
    return torch.nn.functional.silu(x)
def _sigmoid(x):
    return torch.sigmoid(x)
def _layernorm(x, axis=-1, eps=1e-5):
    m = x.mean(dim=axis, keepdim=True)
    v = x.var(dim=axis, keepdim=True, unbiased=False)
    return (x - m) / torch.sqrt(v + eps)
def _scatter_add(tgt, idx, src, axis=0):
    return tgt.clone().index_add_(axis, idx, src)
def _segment_sum(src, seg, n):
    out = torch.zeros((n,) + src.shape[1:], dtype=src.dtype)
    return out.index_add_(0, seg, src)
def _pad(x, pw):
    flat = tuple(v for pair in reversed(pw) for v in pair)
    return torch.nn.functional.pad(x, flat)

kernel_name = 'parallel_sum__grouped_kv_mqa__glu_gated_swiglu__prompt'

def setup_inputs():

    _w = (lambda a: (lambda t: t.to(torch.bfloat16) if t.is_floating_point() else t)(torch.as_tensor(a)))
    def _A_setup():
        B, S, D, H = 1, 2048, 576, 32
        Dh = D // H
        return {
            "x": _w((lambda *s: torch.randn(*s, dtype=torch.bfloat16) * 0.05)(B, S, D)),
            "wq": _w((lambda *s: torch.randn(*s, dtype=torch.bfloat16) * 0.05)(D, H * Dh)),
            "wk": _w((lambda *s: torch.randn(*s, dtype=torch.bfloat16) * 0.05)(D, Dh)),
            "wv": _w((lambda *s: torch.randn(*s, dtype=torch.bfloat16) * 0.05)(D, Dh)),
            "wo": _w((lambda *s: torch.randn(*s, dtype=torch.bfloat16) * 0.05)(H * Dh, D)),
        }
    _A_in = _A_setup()

    def _B_setup():
        B, S, D, F = 1, 2048, 576, 4608
        return {
            "x": _w((lambda *s: torch.randn(*s, dtype=torch.bfloat16) * 0.05)(B, S, D)),
            "w1": _w((lambda *s: torch.randn(*s, dtype=torch.bfloat16) * 0.4)(D, F) * (2.0 / D) ** 0.5),
            "w3": _w((lambda *s: torch.randn(*s, dtype=torch.bfloat16) * 0.4)(D, F) * (2.0 / D) ** 0.5),
            "w2": _w((lambda *s: torch.randn(*s, dtype=torch.bfloat16) * 0.4)(F, D) * (2.0 / F) ** 0.5),
        }
    _B_in = _B_setup()

    return {
        "A_x": _A_in["x"],
        "A_wq": _A_in["wq"],
        "A_wk": _A_in["wk"],
        "A_wv": _A_in["wv"],
        "A_wo": _A_in["wo"],
        "B_x": _B_in["x"],
        "B_w1": _B_in["w1"],
        "B_w3": _B_in["w3"],
        "B_w2": _B_in["w2"],
    }

def reference(A_x, A_wq, A_wk, A_wv, A_wo, B_x, B_w1, B_w3, B_w2):
    def _A_ref(x, wq, wk, wv, wo):
        B, S, D = x.shape
        H = 32
        Dh = D // H
        q = (lambda x, *d: x.permute(*d))((x @ wq).reshape(B, S, H, Dh), 0, 2, 1, 3)
        k = x @ wk
        v = x @ wv
        scores = torch.einsum("bhqd,bkd->bhqk", q, k) * (Dh ** -0.5)
        p = _softmax(scores, axis=-1)
        o = torch.einsum("bhqk,bkd->bhqd", p, v)
        return (lambda x, *d: x.permute(*d))(o, 0, 2, 1, 3).reshape(B, S, H * Dh) @ wo

    def _B_ref(x, w1, w3, w2):
        gate = torch.einsum("bsd,df->bsf", x, w1)
        up = torch.einsum("bsd,df->bsf", x, w3)
        h = _silu(gate) * up
        out = torch.einsum("bsf,fd->bsd", h, w2)
        return out

    x = A_x
    xn = _layernorm(x, axis=-1)
    return x + _A_ref(xn, A_wq, A_wk, A_wv, A_wo) + _B_ref(xn, B_w1, B_w3, B_w2)
```

```cpp
#include <hip/hip_runtime.h>
#include <hip/hip_bf16.h>
#include <stdint.h>

typedef short bf16x8 __attribute__((ext_vector_type(8)));
typedef short bf16x4 __attribute__((ext_vector_type(4)));
typedef float f32x4 __attribute__((ext_vector_type(4)));
typedef float f32x16 __attribute__((ext_vector_type(16)));
typedef unsigned u32x4 __attribute__((ext_vector_type(4)));
typedef unsigned u32x2 __attribute__((ext_vector_type(2)));
typedef unsigned u32x4_a4 __attribute__((ext_vector_type(4), aligned(4)));
typedef __attribute__((address_space(3))) const char* lds_cptr;
typedef __attribute__((address_space(3))) char* lds_ptr;
#define DI __device__ __forceinline__

constexpr int S_ = 2048, D_ = 576, F_ = 4608;
constexpr int NBLK = 324;
constexpr int KV_STAGE = 37888;
constexpr int KV_K2 = 16384, KV_V = 18432;
constexpr size_t WS_XN = 0;
constexpr size_t WS_Q = WS_XN + (size_t)S_ * D_ * 2;
constexpr size_t WS_KV = WS_Q + (size_t)S_ * D_ * 2;
constexpr size_t WS_KMAX = WS_KV + 4 * KV_STAGE;
constexpr size_t WS_H = WS_KMAX + 1024;
constexpr size_t WS_P = WS_H + (size_t)64 * NBLK * 1024;

DI unsigned cvtpk(float lo, float hi) {
  typedef __bf16 b2 __attribute__((ext_vector_type(2)));
  typedef float f2 __attribute__((ext_vector_type(2)));
  f2 v = {lo, hi};
  return __builtin_bit_cast(unsigned, __builtin_convertvector(v, b2));
}
DI float bflo(unsigned u) { return __uint_as_float(u << 16); }
DI float bfhi(unsigned u) { return __uint_as_float(u & 0xffff0000u); }
DI void glds16(const void* sbase, unsigned voff, unsigned lds_addr) {
  asm volatile("s_mov_b32 m0, %0\n\ts_nop 0\n\tglobal_load_lds_dwordx4 %1, %2" ::"s"(lds_addr), "v"(voff), "s"(sbase) : "memory");
}
DI bf16x4 trread(lds_cptr p) {
  return __builtin_amdgcn_ds_read_tr16_b64_v4i16((__attribute__((address_space(3))) bf16x4*)p);
}
DI bf16x8 cat(bf16x4 a, bf16x4 b) { return __builtin_shufflevector(a, b, 0, 1, 2, 3, 4, 5, 6, 7); }
#define MFMA32(a, b, c) __builtin_amdgcn_mfma_f32_32x32x16_bf16((a), (b), (c), 0, 0, 0)
#define MFMA32K8(a, b, c) __builtin_amdgcn_mfma_f32_32x32x8bf16_1k((a), (b), (c), 0, 0, 0)
#define MFMA16(a, b, c) __builtin_amdgcn_mfma_f32_16x16x32_bf16((a), (b), (c), 0, 0, 0)
#define WAIT_VM0() __builtin_amdgcn_s_waitcnt(0x0F70)
#define BARRIER_LGKM() asm volatile("s_waitcnt lgkmcnt(0)\n\ts_barrier" ::: "memory")

constexpr int K1_LDS = 576 * 160;
__global__ __launch_bounds__(256) void k1_ln_qkv(const unsigned short* __restrict__ x, const unsigned short* __restrict__ wq,
                                                 const unsigned short* __restrict__ wk, const unsigned short* __restrict__ wv,
                                                 char* __restrict__ ws) {
  extern __shared__ __attribute__((aligned(16))) char smem[];
  const int tid = threadIdx.x, lane = tid & 63;
  const int w = __builtin_amdgcn_readfirstlane(tid >> 6);
  const int g = blockIdx.x & 7, tg = blockIdx.x >> 3;
  const unsigned lds0 = (unsigned)(uintptr_t)smem;
  const int c = lane & 15, q = lane >> 4;
  const int tok = tg * 64 + w * 16 + c;
  u32x4 xr[18];
  {
    const u32x4* xp = (const u32x4*)(x + (size_t)tok * D_ + 8 * q);
#pragma unroll
    for (int ks = 0; ks < 18; ++ks) xr[ks] = xp[ks * 4];
  }
  if (g < 7) {
    const char* base = (const char*)(wq + 80 * g);
    for (int i = w; i < 90; i += 4) {
      const unsigned ch = i * 64 + lane, pos = ch / 10u, cc = ch - pos * 10u;
      const unsigned k = pos ^ ((pos >> 1) & 4u);
      glds16(base, k * 1152u + cc * 16u, lds0 + i * 1024);
    }
  } else {
    for (int k = tid; k < 576; k += 256) {
      const u32x4* a = (const u32x4*)(wq + (size_t)k * D_ + 560);
      const u32x4 a0 = a[0], a1 = a[1];
      const unsigned* kp = (const unsigned*)(wk + k * 18);
      const unsigned* vp = (const unsigned*)(wv + k * 18);
      const u32x4 k0 = *(const u32x4_a4*)kp, k1 = *(const u32x4_a4*)(kp + 4);
      const unsigned k2 = kp[8];
      const u32x4 v0 = *(const u32x4_a4*)vp, v1 = *(const u32x4_a4*)(vp + 4);
      const unsigned v2 = vp[8];
      char* d = smem + (k ^ ((k >> 1) & 4)) * 160;
      *(u32x4*)(d) = a0; *(u32x4*)(d + 16) = a1;
      *(u32x4*)(d + 32) = k0; *(u32x4*)(d + 48) = k1; *(unsigned*)(d + 64) = k2;
      *(u32x4*)(d + 96) = v0; *(u32x4*)(d + 112) = v1; *(unsigned*)(d + 128) = v2;
    }
  }
  float s = 0.f;
#pragma unroll
  for (int ks = 0; ks < 18; ++ks)
#pragma unroll
    for (int j = 0; j < 4; ++j) s += bflo(xr[ks][j]) + bfhi(xr[ks][j]);
  s += __shfl_xor(s, 16); s += __shfl_xor(s, 32);
  const float mean = s * (1.f / 576.f);
  float v = 0.f;
#pragma unroll
  for (int ks = 0; ks < 18; ++ks)
#pragma unroll
    for (int j = 0; j < 4; ++j) { const float a = bflo(xr[ks][j]) - mean, b = bfhi(xr[ks][j]) - mean; v += a * a + b * b; }
  v += __shfl_xor(v, 16); v += __shfl_xor(v, 32);
  const float rstd = 1.f / sqrtf(v * (1.f / 576.f) + 1e-5f);
#pragma unroll
  for (int ks = 0; ks < 18; ++ks)
#pragma unroll
    for (int j = 0; j < 4; ++j) xr[ks][j] = cvtpk((bflo(xr[ks][j]) - mean) * rstd, (bfhi(xr[ks][j]) - mean) * rstd);
  {
    char* xn = ws + WS_XN + (size_t)(tok >> 5) * 36 * 1024 + ((q >> 1) * 64 + (q & 1) * 32 + (tok & 31)) * 16;
#pragma unroll
    for (int ks = 0; ks < 18; ++ks)
      if ((ks & 7) == g) *(u32x4*)(xn + ks * 2048) = xr[ks];
  }
  WAIT_VM0();
  BARRIER_LGKM();
  f32x4 acc[5];
#pragma unroll
  for (int b = 0; b < 5; ++b) acc[b] = f32x4{0.f, 0.f, 0.f, 0.f};
  const lds_cptr p0 = (lds_cptr)smem + (8 * q + 4 * (q & 1) + (c >> 2)) * 160 + 8 * (c & 3);
  const lds_cptr p1 = (lds_cptr)smem + (8 * q + 4 * ((q & 1) ^ 1) + (c >> 2)) * 160 + 8 * (c & 3);
  constexpr float QSCALE = 0.23570226039551584f * 1.4426950408889634f;
  if (g < 7) {
#pragma unroll
    for (int ks = 0; ks < 18; ++ks)
#pragma unroll
      for (int b = 0; b < 5; ++b) {
        const bf16x8 a = cat(trread(p0 + ks * 5120 + b * 32), trread(p1 + ks * 5120 + b * 32));
        acc[b] = MFMA16(a, __builtin_bit_cast(bf16x8, xr[ks]), acc[b]);
      }
    char* qp = ws + WS_Q + ((size_t)tok * D_ + 80 * g + 4 * q) * 2;
#pragma unroll
    for (int b = 0; b < 5; ++b)
      *(u32x2*)(qp + b * 32) = u32x2{cvtpk(acc[b][0] * QSCALE, acc[b][1] * QSCALE), cvtpk(acc[b][2] * QSCALE, acc[b][3] * QSCALE)};
  } else {
#pragma unroll
    for (int ks = 0; ks < 18; ++ks)
#pragma unroll
      for (int b = 0; b < 5; ++b) {
        const bf16x8 a = cat(trread(p0 + ks * 5120 + b * 32), trread(p1 + ks * 5120 + b * 32));
        if (b < 3) acc[b] = MFMA16(a, __builtin_bit_cast(bf16x8, xr[ks]), acc[b]);
        else acc[b] = MFMA16(__builtin_bit_cast(bf16x8, xr[ks]), a, acc[b]);
      }
    char* qp = ws + WS_Q + ((size_t)tok * D_ + 560 + 4 * q) * 2;
    *(u32x2*)qp = u32x2{cvtpk(acc[0][0] * QSCALE, acc[0][1] * QSCALE), cvtpk(acc[0][2] * QSCALE, acc[0][3] * QSCALE)};
    char* kv = ws + WS_KV + (tok >> 9) * KV_STAGE;
    const int kk = tok & 511;
    const u32x2 kA = {cvtpk(acc[1][0], acc[1][1]), cvtpk(acc[1][2], acc[1][3])};
    const unsigned kB = cvtpk(acc[2][0], acc[2][1]);
    *(u32x2*)(kv + (kk >> 5) * 1024 + ((q >> 1) * 32 + (kk & 31)) * 16 + (q & 1) * 8) = kA;
    if (q == 0) *(unsigned*)(kv + KV_K2 + kk * 4) = kB;
    float n2 = bflo(kA[0]) * bflo(kA[0]) + bfhi(kA[0]) * bfhi(kA[0]) + bflo(kA[1]) * bflo(kA[1]) + bfhi(kA[1]) * bfhi(kA[1]);
    if (q == 0) n2 += bflo(kB) * bflo(kB) + bfhi(kB) * bfhi(kB);
    n2 += __shfl_xor(n2, 16); n2 += __shfl_xor(n2, 32);
    n2 = fmaxf(n2, __shfl_xor(n2, 1)); n2 = fmaxf(n2, __shfl_xor(n2, 2));
    n2 = fmaxf(n2, __shfl_xor(n2, 4)); n2 = fmaxf(n2, __shfl_xor(n2, 8));
    if (lane == 0) ((float*)(ws + WS_KMAX))[tg * 4 + w] = n2;
    const int t0 = (tg * 64 + w * 16) & 511;
    char* vp = kv + KV_V + (t0 >> 4) * 608 + (q & 1) * 304 + (q >> 1) * 8;
    *(u32x2*)(vp + c * 16) = u32x2{cvtpk(acc[3][0], acc[3][1]), cvtpk(acc[3][2], acc[3][3])};
    if (c < 2) *(u32x2*)(vp + (16 + c) * 16) = u32x2{cvtpk(acc[4][0], acc[4][1]), cvtpk(acc[4][2], acc[4][3])};
    if (c == 2) *(u32x2*)(vp + 18 * 16) = u32x2{0x3F803F80u, 0x3F803F80u};
  }
}

constexpr int G1_W3 = 18432 + 128, G1_STAGE = G1_W3 + 18432;
constexpr int K2_FLAG = 2 * KV_STAGE;
constexpr int K2_LDS = 77824;

DI void kv_issue(const char* kvg, unsigned ldsb, int w, int lane) {
#pragma unroll
  for (int i = 0; i < 10; ++i) {
    const int ii = i * 4 + w;
    if (i < 9 || w == 0) glds16(kvg, ii * 1024 + lane * 16, ldsb + ii * 1024);
  }
}

template <bool MAXPASS>
DI void attn_pass(char* smem, const char* kvg, int w, int lane, bf16x8 q1, bf16x4 q2, const f32x16& cinit, f32x16& o, float& mx) {
  const unsigned lds0 = (unsigned)(uintptr_t)smem;
  const int r = lane & 31, h = lane >> 5;
  const int rv = r < 18 ? r : 18;
  kv_issue(kvg, lds0, w, lane);
  for (int st = 0; st < 4; ++st) {
    WAIT_VM0();
    BARRIER_LGKM();
    if (st < 3) kv_issue(kvg + (st + 1) * KV_STAGE, lds0 + ((st + 1) & 1) * KV_STAGE, w, lane);
    const lds_cptr b = (lds_cptr)smem + (st & 1) * KV_STAGE;
    const lds_cptr pk1 = b + lane * 16, pk2 = b + KV_K2 + r * 4, pv = b + KV_V + h * 304 + rv * 16;
#define QK(T) ({ const bf16x8 k1_ = *(const __attribute__((address_space(3))) bf16x8*)(pk1 + (T) * 1024); \
                 const unsigned k2_ = *(const __attribute__((address_space(3))) unsigned*)(pk2 + (T) * 128); \
                 f32x16 s_ = MFMA32K8(__builtin_bit_cast(bf16x4, (u32x2{k2_, 0u})), q2, cinit); \
                 MFMA32(k1_, q1, s_); })
    if (MAXPASS) {
#pragma unroll 2
      for (int t = 0; t < 16; ++t) {
        const f32x16 s = QK(t);
#pragma unroll
        for (int i = 0; i < 16; ++i) mx = fmaxf(mx, s[i]);
      }
    } else {
      f32x16 sc = QK(0);
#pragma unroll
      for (int t = 0; t < 16; ++t) {
        f32x16 sn;
        if (t < 15) sn = QK(t + 1);
        const bf16x8 v0 = *(const __attribute__((address_space(3))) bf16x8*)(pv + (2 * t) * 608);
        const bf16x8 v1 = *(const __attribute__((address_space(3))) bf16x8*)(pv + (2 * t + 1) * 608);
        u32x4 p0, p1;
#pragma unroll
        for (int i = 0; i < 4; ++i) p0[i] = cvtpk(__builtin_amdgcn_exp2f(sc[2 * i]), __builtin_amdgcn_exp2f(sc[2 * i + 1]));
        o = MFMA32(v0, __builtin_bit_cast(bf16x8, p0), o);
#pragma unroll
        for (int i = 0; i < 4; ++i) p1[i] = cvtpk(__builtin_amdgcn_exp2f(sc[8 + 2 * i]), __builtin_amdgcn_exp2f(sc[8 + 2 * i + 1]));
        o = MFMA32(v1, __builtin_bit_cast(bf16x8, p1), o);
        if (t < 15) sc = sn;
      }
    }
#undef QK
  }
}

DI void attn_unit(char* smem, char* ws, int u, int w, int lane) {
  const int gw = u * 4 + w, hd = gw & 31, tb = gw >> 5;
  const int r = lane & 31, h = lane >> 5;
  const char* qrow = ws + WS_Q + ((size_t)(tb * 32 + r) * D_ + 18 * hd) * 2;
  const u32x4 q1 = *(const u32x4_a4*)(qrow + 16 * h);
  unsigned q2 = *(const unsigned*)(qrow + 32);
  if (h) q2 = 0;
  const float* kmax = (const float*)(ws + WS_KMAX);
  float km = fmaxf(kmax[lane], kmax[lane + 64]);
#pragma unroll
  for (int d = 1; d < 64; d <<= 1) km = fmaxf(km, __shfl_xor(km, d));
  float n2 = bflo(q2) * bflo(q2) + bfhi(q2) * bfhi(q2);
#pragma unroll
  for (int j = 0; j < 4; ++j) n2 += bflo(q1[j]) * bflo(q1[j]) + bfhi(q1[j]) * bfhi(q1[j]);
  n2 += __shfl_xor(n2, 32);
  float m = sqrtf(n2 * km) * 1.001f;
  volatile __attribute__((address_space(3))) int* flag = (volatile __attribute__((address_space(3))) int*)((lds_ptr)smem + K2_FLAG);
  if (w == 0 && lane == 0) *flag = 0;
  const char* kvg = ws + WS_KV;
  const bf16x8 qa = __builtin_bit_cast(bf16x8, q1);
  const bf16x4 qb = __builtin_bit_cast(bf16x4, (u32x2{q2, 0u}));
  f32x16 o;
  float l;
  for (int attempt = 0;; ++attempt) {
    f32x16 ci;
#pragma unroll
    for (int i = 0; i < 16; ++i) { ci[i] = -m; o[i] = 0.f; }
    float dummy = 0.f;
    attn_pass<false>(smem, kvg, w, lane, qa, qb, ci, o, dummy);
    l = __shfl(o[10], r);
    if (attempt) break;
    if (!(l >= 8.2718061e-25f)) *flag = 1;
    BARRIER_LGKM();
    const int bad = *flag;
    if (!__builtin_amdgcn_readfirstlane(bad)) break;
#pragma unroll
    for (int i = 0; i < 16; ++i) ci[i] = 0.f;
    float mx = -INFINITY;
    attn_pass<true>(smem, kvg, w, lane, qa, qb, ci, o, mx);
    m = fmaxf(mx, __shfl_xor(mx, 32));
    BARRIER_LGKM();
  }
  const float inv = 1.f / l;
  char* hp = ws + WS_H + (size_t)tb * NBLK * 1024;
  *(u32x4*)(hp + (288 + hd) * 1024 + lane * 16) =
      u32x4{cvtpk(o[0] * inv, o[1] * inv), cvtpk(o[2] * inv, o[3] * inv), cvtpk(o[4] * inv, o[5] * inv), cvtpk(o[6] * inv, o[7] * inv)};
  if (h == 0) {
    const int p = 2 * (hd & 7);
    *(unsigned*)(hp + (320 + (hd >> 3)) * 1024 + (((p >> 2) & 1) * 32 + r) * 16 + 2 * (4 * (p >> 3) + (p & 3))) = cvtpk(o[8] * inv, o[9] * inv);
  }
}

DI float silu_mul(float g, float u) {
  const float e = __builtin_amdgcn_exp2f(g * -1.4426950408889634f);
  return g * __builtin_amdgcn_rcpf(1.f + e) * u;
}

DI void gemm1_unit(char* smem, char* ws, const unsigned short* w1, const unsigned short* w3, int u, int w, int lane) {
  const unsigned lds0 = (unsigned)(uintptr_t)smem;
  const int j = u >> 3, ct = (u & 7) * 4 + (j & 3), rt = j >> 2;
  const int tb = rt * 4 + w;
  const char* wsrc = (const char*)((w >> 1) ? w3 : w1) + (size_t)(w & 1) * 32 * 9216 + ct * 288;
  const unsigned wdst = lds0 + (w >> 1) * G1_W3 + (w & 1) * 9216;
  unsigned woff[9];
#pragma unroll
  for (int i = 0; i < 9; ++i) { const unsigned ch = i * 64 + lane, row = ch / 18u; woff[i] = row * 9216u + (ch - row * 18u) * 16u; }
  const u32x4* xp = (const u32x4*)(ws + WS_XN + (size_t)tb * 36 * 1024 + lane * 16);
  f32x16 acc[9];
#pragma unroll
  for (int b = 0; b < 9; ++b)
#pragma unroll
    for (int i = 0; i < 16; ++i) acc[b][i] = 0.f;
  u32x4 xf[2][4];
#pragma unroll
  for (int i = 0; i < 9; ++i) glds16(wsrc, woff[i], wdst + i * 1024);
#pragma unroll
  for (int i = 0; i < 4; ++i) xf[0][i] = xp[i * 64];
  const int h = lane >> 5, i16 = lane & 15;
  const lds_cptr pa = (lds_cptr)smem + ((lane >> 4) & 1) * G1_W3 + (8 * h + (i16 >> 2)) * 288 + 8 * (i16 & 3);
#pragma unroll
  for (int t = 0; t < 9; ++t) {
    WAIT_VM0();
    BARRIER_LGKM();
    if (t < 8) {
#pragma unroll
      for (int i = 0; i < 9; ++i) glds16(wsrc + (size_t)(t + 1) * 64 * 9216, woff[i], wdst + ((t + 1) & 1) * G1_STAGE + i * 1024);
#pragma unroll
      for (int i = 0; i < 4; ++i) xf[(t + 1) & 1][i] = xp[((t + 1) * 4 + i) * 64];
    }
#pragma unroll
    for (int ks = 0; ks < 4; ++ks)
#pragma unroll
      for (int b = 0; b < 9; ++b) {
        const lds_cptr p = pa + (t & 1) * G1_STAGE + ks * 16 * 288 + b * 32;
        acc[b] = MFMA32(cat(trread(p), trread(p + 4 * 288)), __builtin_bit_cast(bf16x8, xf[t & 1][ks]), acc[b]);
      }
  }
  char* hp = ws + WS_H + ((size_t)tb * NBLK + ct * 9) * 1024 + lane * 16;
#pragma unroll
  for (int b = 0; b < 9; ++b) {
    u32x4 o;
#pragma unroll
    for (int i = 0; i < 4; ++i) o[i] = cvtpk(silu_mul(acc[b][2 * i], acc[b][8 + 2 * i]), silu_mul(acc[b][2 * i + 1], acc[b][9 + 2 * i]));
    *(u32x4*)(hp + b * 1024) = o;
  }
}

#define K2MODE 0
template <int MODE>
__global__ __launch_bounds__(256, 2) void k2_attn_gemm1(const unsigned short* __restrict__ w1, const unsigned short* __restrict__ w3, char* __restrict__ ws) {
  extern __shared__ __attribute__((aligned(16))) char smem[];
  const int lane = threadIdx.x & 63;
  const int w = __builtin_amdgcn_readfirstlane(threadIdx.x >> 6);
  const int u = blockIdx.x;
  if (MODE == 1) { attn_unit(smem, ws, u, w, lane); return; }
  if (MODE == 2) { gemm1_unit(smem, ws, w1, w3, u, w, lane); return; }
  if (u < 256) {
    gemm1_unit(smem, ws, w1, w3, u, w, lane);
    BARRIER_LGKM();
    attn_unit(smem, ws, u, w, lane);
  } else {
    attn_unit(smem, ws, u, w, lane);
    BARRIER_LGKM();
    gemm1_unit(smem, ws, w1, w3, u, w, lane);
  }
}

constexpr int G2_STAGE = 18432 + 16384, G2_NB = 4, K3_LDS = G2_NB * G2_STAGE;

DI void g2_issue(const char* w2, const char* wo, const char* hsrc, int stage, unsigned ldsb, int w, int lane, const unsigned* wrow, const unsigned* wcol, unsigned hoff) {
#pragma unroll
  for (int i = 0; i < 3; ++i) {
    const unsigned dst = ldsb + (i < 2 ? (w * 2 + i) * 1024 : 16384 + w * 256);
    if (stage < 72) {
      if (i < 2 || lane < 16) glds16(w2 + (size_t)stage * 64 * 1152, wrow[i] * 1152u + wcol[i], dst);
    } else {
      const unsigned k = (stage - 72) * 64 + wrow[i], blk = k >> 4, p = k & 15;
      const unsigned row = blk < 32 ? 18 * blk + p : 18 * (8 * (blk - 32) + (p >> 1)) + 16 + (p & 1);
      if (i < 2 || lane < 16) glds16(wo, row * 1152u + wcol[i], dst);
    }
  }
#pragma unroll
  for (int i = 0; i < 2; ++i) glds16(hsrc + (size_t)stage * 4096, hoff + i * 256, ldsb + 18432 + (w * 2 + i) * 1024);
}

__global__ __launch_bounds__(512, 2) void k3_gemm2(const unsigned short* __restrict__ w2, const unsigned short* __restrict__ wo, char* __restrict__ ws) {
  extern __shared__ __attribute__((aligned(16))) char smem[];
  const unsigned lds0 = (unsigned)(uintptr_t)smem;
  const int lane = threadIdx.x & 63;
  const int w = __builtin_amdgcn_readfirstlane(threadIdx.x >> 6);
  const int xcd = blockIdx.x & 7, j = blockIdx.x >> 3;
  const int sp = xcd >> 1, rt = (xcd & 1) * 8 + (j >> 2), ct = j & 3;
  const int st0 = sp == 0 ? 0 : 21 + (sp - 1) * 20, n = sp == 0 ? 21 : 20;
  const int wm = w & 3, kg = w >> 2, tb = rt * 4 + wm;
  const int c = lane & 15, q = lane >> 4;
  unsigned wrow[3], wcol[3];
#pragma unroll
  for (int i = 0; i < 3; ++i) {
    const unsigned ch = i < 2 ? (w * 2 + i) * 64 + lane : 1024 + w * 16 + (lane & 15);
    wrow[i] = ch / 18u; wcol[i] = (ch - wrow[i] * 18u) * 16u + ct * 288;
  }
  const char* hsrc = ws + WS_H + (size_t)tb * NBLK * 1024;
  const unsigned hoff = (2 * kg + (q >> 1)) * 1024 + (q & 1) * 512 + c * 16;
  f32x4 acc[2][9];
#pragma unroll
  for (int i = 0; i < 2; ++i)
#pragma unroll
    for (int b = 0; b < 9; ++b) acc[i][b] = f32x4{0.f, 0.f, 0.f, 0.f};
#pragma unroll
  for (int s = 0; s < 3; ++s) g2_issue((const char*)w2, (const char*)wo, hsrc, st0 + s, lds0 + s * G2_STAGE, w, lane, wrow, wcol, hoff);
  const int aoff = (32 * kg + 16 * (q >> 1) + 4 * (q & 1) + (c >> 2)) * 288 + 8 * (c & 3);
  const int boff = 18432 + w * 2048 + lane * 16;
  for (int t = 0; t < n; ++t) {
    if (t + 2 < n) asm volatile("s_waitcnt vmcnt(10) lgkmcnt(0)\n\ts_barrier" ::: "memory");
    else if (t + 1 < n) asm volatile("s_waitcnt vmcnt(5) lgkmcnt(0)\n\ts_barrier" ::: "memory");
    else asm volatile("s_waitcnt vmcnt(0) lgkmcnt(0)\n\ts_barrier" ::: "memory");
    if (t + 3 < n) g2_issue((const char*)w2, (const char*)wo, hsrc, st0 + t + 3, lds0 + ((t + 3) & 3) * G2_STAGE, w, lane, wrow, wcol, hoff);
    const lds_cptr b = (lds_cptr)smem + (t & 3) * G2_STAGE;
    const bf16x8 h0 = *(const __attribute__((address_space(3))) bf16x8*)(b + boff);
    const bf16x8 h1 = *(const __attribute__((address_space(3))) bf16x8*)(b + boff + 1024);
#pragma unroll
    for (int bb = 0; bb < 9; ++bb) {
      const bf16x8 a = cat(trread(b + aoff + bb * 32), trread(b + aoff + 8 * 288 + bb * 32));
      acc[0][bb] = MFMA16(a, h0, acc[0][bb]);
      acc[1][bb] = MFMA16(a, h1, acc[1][bb]);
    }
  }
  BARRIER_LGKM();
  f32x4* red = (f32x4*)(smem + wm * 18432) + lane;
  if (kg == 1) {
#pragma unroll
    for (int i = 0; i < 2; ++i)
#pragma unroll
      for (int b = 0; b < 9; ++b) red[(i * 9 + b) * 64] = acc[i][b];
  }
  BARRIER_LGKM();
  if (kg == 0) {
    float* pp = (float*)(ws + WS_P) + ((size_t)sp * S_ + tb * 32 + c) * D_ + ct * 144 + 4 * q;
#pragma unroll
    for (int i = 0; i < 2; ++i)
#pragma unroll
      for (int b = 0; b < 9; ++b) *(f32x4*)(pp + (size_t)i * 16 * D_ + b * 16) = acc[i][b] + red[(i * 9 + b) * 64];
  }
}

__global__ __launch_bounds__(256) void k4_reduce(const unsigned short* __restrict__ x, const char* __restrict__ ws, unsigned short* __restrict__ out) {
  const size_t i = ((size_t)blockIdx.x * 256 + threadIdx.x) * 8;
  const float* p = (const float*)(ws + WS_P) + i;
  const u32x4 xv = *(const u32x4*)(x + i);
  f32x4 a = *(const f32x4*)p, b = *(const f32x4*)(p + 4);
#pragma unroll
  for (int s = 1; s < 4; ++s) { a += *(const f32x4*)(p + (size_t)s * S_ * D_); b += *(const f32x4*)(p + (size_t)s * S_ * D_ + 4); }
  *(u32x4*)(out + i) = u32x4{cvtpk(a[0] + bflo(xv[0]), a[1] + bfhi(xv[0])), cvtpk(a[2] + bflo(xv[1]), a[3] + bfhi(xv[1])),
                             cvtpk(b[0] + bflo(xv[2]), b[1] + bfhi(xv[2])), cvtpk(b[2] + bflo(xv[3]), b[3] + bfhi(xv[3]))};
}

extern "C" void kernel_launch(void* const* d_in, const int* in_sizes, int n_in, void* d_out, int out_size, void* d_ws, size_t ws_size,
                              hipStream_t stream) {
  const unsigned short* x = (const unsigned short*)d_in[0];
  const unsigned short* wq = (const unsigned short*)d_in[1];
  const unsigned short* wk = (const unsigned short*)d_in[2];
  const unsigned short* wv = (const unsigned short*)d_in[3];
  const unsigned short* wo = (const unsigned short*)d_in[4];
  const unsigned short* w1 = (const unsigned short*)d_in[6];
  const unsigned short* w3 = (const unsigned short*)d_in[7];
  const unsigned short* w2 = (const unsigned short*)d_in[8];
  char* ws = (char*)d_ws;
  (void)hipFuncSetAttribute((const void*)k1_ln_qkv, hipFuncAttributeMaxDynamicSharedMemorySize, K1_LDS);
  (void)hipFuncSetAttribute((const void*)k3_gemm2, hipFuncAttributeMaxDynamicSharedMemorySize, K3_LDS);
  k1_ln_qkv<<<256, 256, K1_LDS, stream>>>(x, wq, wk, wv, ws);
  (void)hipFuncSetAttribute((const void*)k2_attn_gemm1<0>, hipFuncAttributeMaxDynamicSharedMemorySize, K2_LDS);
  k2_attn_gemm1<0><<<512, 256, K2_LDS, stream>>>(w1, w3, ws);
  k3_gemm2<<<256, 512, K3_LDS, stream>>>(w2, wo, ws);
  k4_reduce<<<S_ * D_ / 8 / 256, 256, 0, stream>>>(x, ws, (unsigned short*)d_out);
}
```

```cpp
#include <hip/hip_runtime.h>
#include <hip/hip_bf16.h>
#include <stdint.h>

typedef short bf16x8 __attribute__((ext_vector_type(8)));
typedef short bf16x4 __attribute__((ext_vector_type(4)));
typedef float f32x4 __attribute__((ext_vector_type(4)));
typedef float f32x16 __attribute__((ext_vector_type(16)));
typedef unsigned u32x4 __attribute__((ext_vector_type(4)));
typedef unsigned u32x2 __attribute__((ext_vector_type(2)));
typedef unsigned u32x4_a4 __attribute__((ext_vector_type(4), aligned(4)));
typedef __attribute__((address_space(3))) const char* lds_cptr;
typedef __attribute__((address_space(3))) char* lds_ptr;
#define DI __device__ __forceinline__

#define K2MODE 0
constexpr int S_ = 2048, D_ = 576, F_ = 4608;
constexpr int NBLK = 324;
constexpr int KV_HS = 20224, KV_HSG = 20480;
constexpr int KV_K2 = 8192, KV_V = 10240;
constexpr size_t WS_XN = 0;
constexpr size_t WS_Q = WS_XN + (size_t)S_ * D_ * 2;
constexpr size_t WS_KV = WS_Q + (size_t)S_ * D_ * 2;
constexpr size_t WS_KMAX = WS_KV + 8 * KV_HSG;
constexpr size_t WS_WO = WS_KMAX + 1024;
constexpr size_t WS_H = WS_WO + (size_t)D_ * D_ * 2;
constexpr size_t WS_P = WS_H + (size_t)64 * NBLK * 1024;

DI unsigned cvtpk(float lo, float hi) {
  typedef __bf16 b2 __attribute__((ext_vector_type(2)));
  typedef float f2 __attribute__((ext_vector_type(2)));
  f2 v = {lo, hi};
  return __builtin_bit_cast(unsigned, __builtin_convertvector(v, b2));
}
DI float bflo(unsigned u) { return __uint_as_float(u << 16); }
DI float bfhi(unsigned u) { return __uint_as_float(u & 0xffff0000u); }
DI void glds16(const void* sbase, unsigned voff, unsigned lds_addr) {
  asm volatile("s_mov_b32 m0, %0\n\ts_nop 0\n\tglobal_load_lds_dwordx4 %1, %2" ::"s"(lds_addr), "v"(voff), "s"(sbase) : "memory");
}
DI int negmask(int x) { int m; asm("s_ashr_i32 %0, %1, 31" : "=s"(m) : "s"(x) : "scc"); return m; }
DI void glds16m(const void* sbase, unsigned voff, unsigned lds_addr, int mlo, int mhi) {
  asm volatile("s_mov_b32 m0, %0\n\ts_mov_b32 exec_lo, %3\n\ts_mov_b32 exec_hi, %4\n\tglobal_load_lds_dwordx4 %1, %2\n\ts_mov_b64 exec, -1"
               ::"s"(lds_addr), "v"(voff), "s"(sbase), "s"(mlo), "s"(mhi) : "memory");
}
template <int OFF2>
DI void gload2m(u32x4& a, u32x4& b, const void* sbase, unsigned voff, int m) {
  asm volatile("s_mov_b32 exec_lo, %4\n\ts_mov_b32 exec_hi, %4\n\tglobal_load_dwordx4 %0, %2, %3\n\tglobal_load_dwordx4 %1, %2, %3 offset:%5\n\ts_mov_b64 exec, -1"
               : "+v"(a), "+v"(b) : "v"(voff), "s"(sbase), "s"(m), "i"(OFF2) : "memory");
}

DI bf16x4 trread(lds_cptr p) {
  return __builtin_amdgcn_ds_read_tr16_b64_v4i16((__attribute__((address_space(3))) bf16x4*)p);
}
DI bf16x8 cat(bf16x4 a, bf16x4 b) { return __builtin_shufflevector(a, b, 0, 1, 2, 3, 4, 5, 6, 7); }
#define MFMA32(a, b, c) __builtin_amdgcn_mfma_f32_32x32x16_bf16((a), (b), (c), 0, 0, 0)
#define MFMA32K8(a, b, c) __builtin_amdgcn_mfma_f32_32x32x8bf16_1k((a), (b), (c), 0, 0, 0)
#define MFMA16(a, b, c) __builtin_amdgcn_mfma_f32_16x16x32_bf16((a), (b), (c), 0, 0, 0)
#define WAIT_VM0() __builtin_amdgcn_s_waitcnt(0x0F70)
#define BARRIER_LGKM() asm volatile("s_waitcnt lgkmcnt(0)\n\ts_barrier" ::: "memory")
#define STAMP_AT(kid, slot)
#define STAMP_DECL
#define STAMP_R(slot)
#define STAMP_FLUSH(kid, lo, hi)

constexpr int K1_LDS = 576 * 160;
__global__ __launch_bounds__(256) void k1_ln_qkv(const unsigned short* __restrict__ x, const unsigned short* __restrict__ wq,
                                                 const unsigned short* __restrict__ wk, const unsigned short* __restrict__ wv,
                                                 const unsigned short* __restrict__ wo, char* __restrict__ ws) {
  extern __shared__ __attribute__((aligned(16))) char smem[];
  const int tid = threadIdx.x, lane = tid & 63;
  const int w = __builtin_amdgcn_readfirstlane(tid >> 6);
  const int g = blockIdx.x & 7, tg = blockIdx.x >> 3;
  const unsigned lds0 = (unsigned)(uintptr_t)smem;
  const int c = lane & 15, q = lane >> 4;
  const int tok = tg * 64 + w * 16 + c;
  STAMP_AT(0, 0);
  u32x4 xr[18];
  {
    const u32x4* xp = (const u32x4*)(x + (size_t)tok * D_ + 8 * q);
#pragma unroll
    for (int ks = 0; ks < 18; ++ks) xr[ks] = xp[ks * 4];
  }
  if (g < 7) {
    const char* base = (const char*)(wq + 80 * g);
    for (int i = w; i < 90; i += 4) {
      const unsigned ch = i * 64 + lane, pos = ch / 10u, cc = ch - pos * 10u;
      const unsigned k = pos ^ ((pos >> 1) & 4u);
      glds16(base, k * 1152u + cc * 16u, lds0 + i * 1024);
    }
  } else {
    for (int k = tid; k < 576; k += 256) {
      const u32x4* a = (const u32x4*)(wq + (size_t)k * D_ + 560);
      const u32x4 a0 = a[0], a1 = a[1];
      const unsigned* kp = (const unsigned*)(wk + k * 18);
      const unsigned* vp = (const unsigned*)(wv + k * 18);
      const u32x4 k0 = *(const u32x4_a4*)kp, k1 = *(const u32x4_a4*)(kp + 4);
      const unsigned k2 = kp[8];
      const u32x4 v0 = *(const u32x4_a4*)vp, v1 = *(const u32x4_a4*)(vp + 4);
      const unsigned v2 = vp[8];
      char* d = smem + (k ^ ((k >> 1) & 4)) * 160;
      *(u32x4*)(d) = a0; *(u32x4*)(d + 16) = a1;
      *(u32x4*)(d + 32) = k0; *(u32x4*)(d + 48) = k1; *(unsigned*)(d + 64) = k2;
      *(u32x4*)(d + 96) = v0; *(u32x4*)(d + 112) = v1; *(unsigned*)(d + 128) = v2;
    }
  }
  if (tid < 162) {
    const unsigned ch = blockIdx.x * 162 + tid, k = ch / 72u, cc = ch - k * 72u, blk = k >> 4, p = k & 15;
    const unsigned row = blk < 32 ? 18 * blk + p : 18 * (8 * (blk - 32) + (p >> 1)) + 16 + (p & 1);
    *(u32x4*)(ws + WS_WO + ch * 16) = *(const u32x4*)((const char*)wo + row * 1152u + cc * 16u);
  }
  float s = 0.f;
#pragma unroll
  for (int ks = 0; ks < 18; ++ks)
#pragma unroll
    for (int j = 0; j < 4; ++j) s += bflo(xr[ks][j]) + bfhi(xr[ks][j]);
  s += __shfl_xor(s, 16); s += __shfl_xor(s, 32);
  const float mean = s * (1.f / 576.f);
  float v = 0.f;
#pragma unroll
  for (int ks = 0; ks < 18; ++ks)
#pragma unroll
    for (int j = 0; j < 4; ++j) { const float a = bflo(xr[ks][j]) - mean, b = bfhi(xr[ks][j]) - mean; v += a * a + b * b; }
  v += __shfl_xor(v, 16); v += __shfl_xor(v, 32);
  const float rstd = 1.f / sqrtf(v * (1.f / 576.f) + 1e-5f);
#pragma unroll
  for (int ks = 0; ks < 18; ++ks)
#pragma unroll
    for (int j = 0; j < 4; ++j) xr[ks][j] = cvtpk((bflo(xr[ks][j]) - mean) * rstd, (bfhi(xr[ks][j]) - mean) * rstd);
  {
    char* xn = ws + WS_XN + (size_t)(tok >> 5) * 36 * 1024 + ((q >> 1) * 64 + (q & 1) * 32 + (tok & 31)) * 16;
#pragma unroll
    for (int ks = 0; ks < 18; ++ks)
      if ((ks & 7) == g) *(u32x4*)(xn + ks * 2048) = xr[ks];
  }
  STAMP_AT(0, 1);
  WAIT_VM0();
  BARRIER_LGKM();
  STAMP_AT(0, 2);
  f32x4 acc[5];
#pragma unroll
  for (int b = 0; b < 5; ++b) acc[b] = f32x4{0.f, 0.f, 0.f, 0.f};
  const lds_cptr p0 = (lds_cptr)smem + (8 * q + 4 * (q & 1) + (c >> 2)) * 160 + 8 * (c & 3);
  const lds_cptr p1 = (lds_cptr)smem + (8 * q + 4 * ((q & 1) ^ 1) + (c >> 2)) * 160 + 8 * (c & 3);
  constexpr float QSCALE = 0.23570226039551584f * 1.4426950408889634f;
  if (g < 7) {
#pragma unroll
    for (int ks = 0; ks < 18; ++ks)
#pragma unroll
      for (int b = 0; b < 5; ++b) {
        const bf16x8 a = cat(trread(p0 + ks * 5120 + b * 32), trread(p1 + ks * 5120 + b * 32));
        acc[b] = MFMA16(a, __builtin_bit_cast(bf16x8, xr[ks]), acc[b]);
      }
    char* qp = ws + WS_Q + ((size_t)tok * D_ + 80 * g + 4 * q) * 2;
#pragma unroll
    for (int b = 0; b < 5; ++b)
      *(u32x2*)(qp + b * 32) = u32x2{cvtpk(acc[b][0] * QSCALE, acc[b][1] * QSCALE), cvtpk(acc[b][2] * QSCALE, acc[b][3] * QSCALE)};
  } else {
#pragma unroll
    for (int ks = 0; ks < 18; ++ks)
#pragma unroll
      for (int b = 0; b < 5; ++b) {
        const bf16x8 a = cat(trread(p0 + ks * 5120 + b * 32), trread(p1 + ks * 5120 + b * 32));
        if (b < 3) acc[b] = MFMA16(a, __builtin_bit_cast(bf16x8, xr[ks]), acc[b]);
        else acc[b] = MFMA16(__builtin_bit_cast(bf16x8, xr[ks]), a, acc[b]);
      }
    char* qp = ws + WS_Q + ((size_t)tok * D_ + 560 + 4 * q) * 2;
    *(u32x2*)qp = u32x2{cvtpk(acc[0][0] * QSCALE, acc[0][1] * QSCALE), cvtpk(acc[0][2] * QSCALE, acc[0][3] * QSCALE)};
    char* kv = ws + WS_KV + (tok >> 8) * KV_HSG;
    const int kk = tok & 255;
    const u32x2 kA = {cvtpk(acc[1][0], acc[1][1]), cvtpk(acc[1][2], acc[1][3])};
    const unsigned kB = cvtpk(acc[2][0], acc[2][1]);
    *(u32x2*)(kv + (kk >> 5) * 1024 + ((q >> 1) * 32 + (kk & 31)) * 16 + (q & 1) * 8) = kA;
    if (q == 0) *(u32x2*)(kv + KV_K2 + kk * 8) = u32x2{kB, 0x3F803F80u};
    float n2 = bflo(kA[0]) * bflo(kA[0]) + bfhi(kA[0]) * bfhi(kA[0]) + bflo(kA[1]) * bflo(kA[1]) + bfhi(kA[1]) * bfhi(kA[1]);
    if (q == 0) n2 += bflo(kB) * bflo(kB) + bfhi(kB) * bfhi(kB);
    n2 += __shfl_xor(n2, 16); n2 += __shfl_xor(n2, 32);
    n2 = fmaxf(n2, __shfl_xor(n2, 1)); n2 = fmaxf(n2, __shfl_xor(n2, 2));
    n2 = fmaxf(n2, __shfl_xor(n2, 4)); n2 = fmaxf(n2, __shfl_xor(n2, 8));
    if (lane == 0) ((float*)(ws + WS_KMAX))[tg * 4 + w] = n2;
    const int t0 = (tg * 64 + w * 16) & 255;
    char* vp = kv + KV_V + (t0 >> 4) * 624 + (q & 1) * 304 + (q >> 1) * 8;
    *(u32x2*)(vp + c * 16) = u32x2{cvtpk(acc[3][0], acc[3][1]), cvtpk(acc[3][2], acc[3][3])};
    if (c < 2) *(u32x2*)(vp + (16 + c) * 16) = u32x2{cvtpk(acc[4][0], acc[4][1]), cvtpk(acc[4][2], acc[4][3])};
    if (c == 2) *(u32x2*)(vp + 18 * 16) = u32x2{0x3F803F80u, 0x3F803F80u};
    if (c == 3 && (q & 1)) *(u32x2*)(vp + 19 * 16) = u32x2{0u, 0u};
  }
  STAMP_AT(0, 3);
}

constexpr int K2_FLAG = 4 * KV_HS;
constexpr int K2_LDS = K2_FLAG + 16;
#define SBAR() __builtin_amdgcn_sched_barrier(0)
#define PIN(x) asm volatile("" : "+v"(x))
#define LDS128(p) (*(const __attribute__((address_space(3))) bf16x8*)(p))
#define LDS64(p) (*(const __attribute__((address_space(3))) bf16x4*)(p))

DI void kv_issue(const char* src, unsigned va, unsigned vb, unsigned dst, int m, int m5hi) {
  asm volatile("s_mov_b32 m0, %0\n\ts_mov_b32 exec_lo, %4\n\ts_mov_b32 exec_hi, %4\n\t"
               "global_load_lds_dwordx4 %1, %3\n\tglobal_load_lds_dwordx4 %1, %3 offset:1024\n\t"
               "global_load_lds_dwordx4 %1, %3 offset:2048\n\tglobal_load_lds_dwordx4 %1, %3 offset:3072\n\t"
               "s_add_u32 m0, %0, 0x1000\n\ts_mov_b32 exec_hi, %5\n\tglobal_load_lds_dwordx4 %2, %3\n\ts_mov_b64 exec, -1"
               ::"s"(dst), "v"(va), "v"(vb), "s"(src), "s"(m), "s"(m5hi) : "memory", "scc");
}
DI bf16x4 q_tail(unsigned q2, float m, int h) {
  const float m1 = bflo(cvtpk(m, 0.f)), r1 = m - m1, m2 = bflo(cvtpk(r1, 0.f)), m3 = r1 - m2;
  return __builtin_bit_cast(bf16x4, h ? u32x2{0u, cvtpk(-m3, 0.f)} : u32x2{q2, cvtpk(-m1, -m2)});
}

DI void kv_first(char* smem, const char* kvg, int w, int lane) {
  const unsigned va = w * 5120 + lane * 16;
#pragma unroll
  for (int i = 0; i < 3; ++i) kv_issue(kvg + i * KV_HSG, va, va + 4096, (unsigned)(uintptr_t)smem + w * 5120 + i * KV_HS, -1, w == 3 ? 0xFFFF : -1);
}

#define APRIO_HI 2
#define APRIO_LO 0
DI void attn_main(char* ws, char* smem, const char* kvg, int w, int lane, bf16x8 qa1, bf16x4 qa2, bf16x8 qb1, bf16x4 qb2, f32x16& oa, f32x16& ob) {
  const unsigned lds0 = (unsigned)(uintptr_t)smem;
  const int r = lane & 31, h = lane >> 5;
  const unsigned va = w * 5120 + lane * 16, vb = va + 4096, dst = lds0 + w * 5120;
  const int m5 = w == 3 ? 0xFFFF : -1;
  const int ok1 = lane * 16, ok2 = KV_K2 + r * 8, ov = KV_V + (r < 19 ? h * 304 + r * 16 : 608);
  const f32x16 zero = {};
  STAMP_DECL;
  STAMP_R(2);
  asm volatile("s_waitcnt vmcnt(10)\n\ts_barrier" ::: "memory");
  STAMP_R(3);
  bf16x8 k1 = LDS128((lds_cptr)smem + ok1), v0 = LDS128((lds_cptr)smem + ov), v1 = {};
  bf16x4 k2 = LDS64((lds_cptr)smem + ok2);
  f32x16 sa = MFMA32(k1, qa1, MFMA32K8(k2, qa2, zero)), sb;
  u32x4 p0, p1 = {0u, 0u, 0u, 0u};
  float e2 = 0.f, e3 = 0.f;
#define SLOT(MF, RD, NEXT, S, i, PP, jp, P, j) do { \
    MF; SBAR(); __builtin_amdgcn_s_setprio(APRIO_LO); SBAR(); \
    float e0 = __builtin_amdgcn_exp2f(S[i]); PIN(e0); SBAR(); RD; SBAR(); \
    float e1 = __builtin_amdgcn_exp2f(S[i + 1]); PIN(e1); SBAR(); \
    PP[jp] = cvtpk(e2, e3); PIN(PP); SBAR(); \
    e2 = __builtin_amdgcn_exp2f(S[i + 2]); PIN(e2); SBAR(); PIN(NEXT); SBAR(); \
    e3 = __builtin_amdgcn_exp2f(S[i + 3]); PIN(e3); SBAR(); __builtin_amdgcn_s_setprio(APRIO_HI); SBAR(); \
    P[j] = cvtpk(e0, e1); PIN(P); SBAR(); } while (0)
#define PB(x) __builtin_bit_cast(bf16x8, x)
  for (int hs = 0; hs < 8; ++hs) {
    const lds_cptr cur = (lds_cptr)smem + (hs & 3) * KV_HS, nxt = (lds_cptr)smem + ((hs + 1) & 3) * KV_HS;
    const lds_cptr ck1 = cur + ok1, ck2 = cur + ok2, cv = cur + ov, nk1 = nxt + ok1, nk2 = nxt + ok2, nv = nxt + ov;
#pragma unroll
    for (int t = 0; t < 8; ++t) {
      if (t == 7) {
        asm volatile("s_waitcnt vmcnt(5)\n\ts_barrier" ::: "memory");
        const int m = negmask(hs - 5);
        kv_issue(kvg + ((hs + 3) & 7) * KV_HSG, va, vb, dst + ((hs + 3) & 3) * KV_HS, m, m & m5);
      }
      SBAR();
      SLOT(sb = MFMA32K8(k2, qb2, zero), k2 = t < 7 ? LDS64(ck2 + (t + 1) * 256) : LDS64(nk2), e0, sa, 0, p1, 3, p0, 0);
      SLOT(ob = MFMA32(v1, PB(p1), ob), v1 = LDS128(cv + (2 * t + 1) * 624), e0, sa, 4, p0, 1, p0, 2);
      SLOT(sb = MFMA32(k1, qb1, sb), k1 = t < 7 ? LDS128(ck1 + (t + 1) * 1024) : LDS128(nk1), v0, sa, 8, p0, 3, p1, 0);
      SLOT(oa = MFMA32(v0, PB(p0), oa), , k2, sa, 12, p1, 1, p1, 2);
      SLOT(sa = MFMA32K8(k2, qa2, zero), , v1, sb, 0, p1, 3, p0, 0);
      SLOT(oa = MFMA32(v1, PB(p1), oa), , k1, sb, 4, p0, 1, p0, 2);
      SLOT(sa = MFMA32(k1, qa1, sa), , e0, sb, 8, p0, 3, p1, 0);
      SLOT(ob = MFMA32(v0, PB(p0), ob), v0 = t < 7 ? LDS128(cv + (2 * t + 2) * 624) : LDS128(nv), e0, sb, 12, p1, 1, p1, 2);
    }
  }
#undef SLOT
  STAMP_R(4);
  STAMP_FLUSH(1, 2, 4);
  p1[3] = cvtpk(e2, e3);
  ob = MFMA32(v1, PB(p1), ob);
#undef PB
  asm volatile("s_waitcnt vmcnt(0) lgkmcnt(0)\n\ts_barrier" ::: "memory");
}

DI void attn_max(char* smem, const char* kvg, int w, int lane, bf16x8 qa1, bf16x4 qa2, bf16x8 qb1, bf16x4 qb2, float& ma, float& mb) {
  const unsigned lds0 = (unsigned)(uintptr_t)smem;
  const unsigned va = w * 5120 + lane * 16;
  const f32x16 zero = {};
  for (int hs = 0; hs < 8; ++hs) {
    kv_issue(kvg + hs * KV_HSG, va, va + 4096, lds0 + w * 5120, -1, w == 3 ? 0xFFFF : -1);
    asm volatile("s_waitcnt vmcnt(0)\n\ts_barrier" ::: "memory");
#pragma unroll 1
    for (int t = 0; t < 8; ++t) {
      const bf16x8 k1 = LDS128((lds_cptr)smem + lane * 16 + t * 1024);
      const bf16x4 k2 = LDS64((lds_cptr)smem + KV_K2 + (lane & 31) * 8 + t * 256);
      const f32x16 sa = MFMA32(k1, qa1, MFMA32K8(k2, qa2, zero)), sb = MFMA32(k1, qb1, MFMA32K8(k2, qb2, zero));
#pragma unroll
      for (int i = 0; i < 16; ++i) { ma = fmaxf(ma, sa[i]); mb = fmaxf(mb, sb[i]); }
    }
    BARRIER_LGKM();
  }
  ma = fmaxf(ma, __shfl_xor(ma, 32));
  mb = fmaxf(mb, __shfl_xor(mb, 32));
}

DI void attn_store(char* hp, int hd, int lane, const f32x16& o, float l) {
  const float inv = 1.f / l;
  *(u32x4*)(hp + (288 + hd) * 1024 + lane * 16) =
      u32x4{cvtpk(o[0] * inv, o[1] * inv), cvtpk(o[2] * inv, o[3] * inv), cvtpk(o[4] * inv, o[5] * inv), cvtpk(o[6] * inv, o[7] * inv)};
  if (lane < 32) {
    const int p = 2 * (hd & 7);
    *(unsigned*)(hp + (320 + (hd >> 3)) * 1024 + (((p >> 2) & 1) * 32 + lane) * 16 + 2 * (4 * (p >> 3) + (p & 3))) = cvtpk(o[8] * inv, o[9] * inv);
  }
}

DI float half_sum(float v) {
  const auto t = __builtin_amdgcn_permlane32_swap(__float_as_uint(v), __float_as_uint(v), false, false);
  return __uint_as_float(t[0]) + __uint_as_float(t[1]);
}
DI void attn_unit(char* smem, char* ws, int u, int w, int lane) {
  const char* kvg = ws + WS_KV;
  kv_first(smem, kvg, w, lane);
  const int gw = u * 4 + w, hd = (gw & 15) * 2, tb = gw >> 4;
  const int r = lane & 31, h = lane >> 5;
  const char* qrow = ws + WS_Q + ((size_t)(tb * 32 + r) * D_ + 18 * hd) * 2;
  const u32x4 qa = *(const u32x4_a4*)(qrow + 16 * h), qb = *(const u32x4_a4*)(qrow + 36 + 16 * h);
  const unsigned qa2 = *(const unsigned*)(qrow + 32), qb2 = *(const unsigned*)(qrow + 68);
  const u32x2 kx = *(const u32x2*)(ws + WS_KMAX + lane * 8);
  unsigned ku = max(kx[0], kx[1]);
  ku = max(ku, (unsigned)__builtin_amdgcn_update_dpp(0, (int)ku, 0xB1, 0xF, 0xF, true));
  ku = max(ku, (unsigned)__builtin_amdgcn_update_dpp(0, (int)ku, 0x4E, 0xF, 0xF, true));
  ku = max(ku, (unsigned)__builtin_amdgcn_update_dpp(0, (int)ku, 0x141, 0xF, 0xF, true));
  ku = max(ku, (unsigned)__builtin_amdgcn_update_dpp(0, (int)ku, 0x140, 0xF, 0xF, true));
  const float km = __uint_as_float(max(max((unsigned)__builtin_amdgcn_readlane(ku, 0), (unsigned)__builtin_amdgcn_readlane(ku, 16)),
                                       max((unsigned)__builtin_amdgcn_readlane(ku, 32), (unsigned)__builtin_amdgcn_readlane(ku, 48))));
  float na = 0.f, nb = 0.f;
#pragma unroll
  for (int j = 0; j < 4; ++j) {
    na += bflo(qa[j]) * bflo(qa[j]) + bfhi(qa[j]) * bfhi(qa[j]);
    nb += bflo(qb[j]) * bflo(qb[j]) + bfhi(qb[j]) * bfhi(qb[j]);
  }
  na = half_sum(na) + bflo(qa2) * bflo(qa2) + bfhi(qa2) * bfhi(qa2);
  nb = half_sum(nb) + bflo(qb2) * bflo(qb2) + bfhi(qb2) * bfhi(qb2);
  float ma = sqrtf(na * km) * 1.001f, mb = sqrtf(nb * km) * 1.001f;
  volatile __attribute__((address_space(3))) int* flag = (volatile __attribute__((address_space(3))) int*)((lds_ptr)smem + K2_FLAG);
  if (w == 0 && lane == 0) *flag = 0;
  const bf16x8 qa1 = __builtin_bit_cast(bf16x8, qa), qb1 = __builtin_bit_cast(bf16x8, qb);
  f32x16 oa, ob;
  float la, lb;
  for (int attempt = 0;; ++attempt) {
#pragma unroll
    for (int i = 0; i < 16; ++i) { oa[i] = 0.f; ob[i] = 0.f; }
    attn_main(ws, smem, kvg, w, lane, qa1, q_tail(qa2, ma, h), qb1, q_tail(qb2, mb, h), oa, ob);
    la = __shfl(oa[10], r); lb = __shfl(ob[10], r);
    if (attempt) break;
    if (!(la >= 8.2718061e-25f && lb >= 8.2718061e-25f)) *flag = 1;
    BARRIER_LGKM();
    const int bad = *flag;
    if (!__builtin_amdgcn_readfirstlane(bad)) break;
    ma = -INFINITY; mb = -INFINITY;
    attn_max(smem, kvg, w, lane, qa1, q_tail(qa2, 0.f, h), qb1, q_tail(qb2, 0.f, h), ma, mb);
    kv_first(smem, kvg, w, lane);
  }
  char* hp = ws + WS_H + (size_t)tb * NBLK * 1024;
  attn_store(hp, hd, lane, oa, la);
  attn_store(hp, hd + 1, lane, ob, lb);
}

DI float silu_mul(float g, float u) {
  const float e = __builtin_amdgcn_exp2f(g * -1.4426950408889634f);
  return g * __builtin_amdgcn_rcpf(1.f + e) * u;
}

constexpr int G1_W3 = 9216 + 128, G1_SLOT = G1_W3 + 9216;
DI void glds16h(const void* sbase, unsigned voff, unsigned lds_addr) {
  asm volatile("s_mov_b32 m0, %0\n\ts_mov_b32 exec_hi, 0\n\tglobal_load_lds_dwordx4 %1, %2\n\ts_mov_b32 exec_hi, -1" ::"s"(lds_addr), "v"(voff), "s"(sbase) : "memory");
}
template <int OFF>
DI void gload16(u32x4& a, const void* sbase, unsigned voff) {
  asm volatile("global_load_dwordx4 %0, %1, %2 offset:%3" : "+v"(a) : "v"(voff), "s"(sbase), "i"(OFF) : "memory");
}

DI void gemm1_unit(char* smem, char* ws, const unsigned short* w1, const unsigned short* w3, const unsigned short* w2, int u, int w, int lane) {
  const unsigned lds0 = (unsigned)(uintptr_t)smem;
  const int j = u >> 3, ct = (u & 7) * 4 + (j & 3), rt = j >> 2;
  const char* wsrc = (const char*)((w >> 1) ? w3 : w1) + (w & 1) * 16 * 9216 + ct * 288;
  const unsigned wdst = lds0 + (w >> 1) * G1_W3 + (w & 1) * 4608;
  unsigned woff[5];
#pragma unroll
  for (int i = 0; i < 5; ++i) { const unsigned ch = i * 64 + (i < 4 ? lane : lane & 31), row = ch / 18u; woff[i] = row * 9216u + (ch - row * 18u) * 16u; }
  const char* xsrc = ws + WS_XN + (size_t)(rt * 4 + w) * 36864;
  const unsigned xoff = lane * 16;
  int sw = rt * 9 / 4, sx = sw;
  u32x4 xf[3][2];
#pragma unroll
  for (int i = 0; i < 3; ++i) { xf[i][0] = u32x4{0, 0, 0, 0}; xf[i][1] = u32x4{0, 0, 0, 0}; }
#define G1_WP(slot, i) do { if ((i) < 4) glds16(wsrc + (size_t)sw * 294912, woff[i], wdst + (slot) * G1_SLOT + (i) * 1024); \
    else { glds16h(wsrc + (size_t)sw * 294912, woff[4], wdst + (slot) * G1_SLOT + 4096); sw = sw == 17 ? 0 : sw + 1; } } while (0)
#define G1_XP(set, i) do { if ((i) == 0) gload16<0>(xf[set][0], xsrc + sx * 2048, xoff); \
    else { gload16<1024>(xf[set][1], xsrc + sx * 2048, xoff); sx = sx == 17 ? 0 : sx + 1; } } while (0)
#pragma unroll
  for (int s = 0; s < 3; ++s) {
#pragma unroll
    for (int i = 0; i < 5; ++i) G1_WP(s, i);
    if (s) { G1_XP(s - 1, 0); G1_XP(s - 1, 1); }
  }
  const int h = lane >> 5, i16 = lane & 15;
  const int aoff = ((lane >> 4) & 1) * G1_W3 + (8 * h + (i16 >> 2)) * 288 + 8 * (i16 & 3);
#define G1_A(p, i) cat(trread((p) + ((i) / 9) * 4608 + ((i) % 9) * 32), trread((p) + ((i) / 9) * 4608 + ((i) % 9) * 32 + 4 * 288))
  bf16x8 af[6];
  STAMP_DECL;
  STAMP_R(2);
  asm volatile("s_waitcnt vmcnt(14)\n\ts_barrier" ::: "memory");
  STAMP_R(3);
#pragma unroll
  for (int i = 0; i < 6; ++i) af[i] = G1_A((lds_cptr)smem + aoff, i);
  int g = 0;
  f32x16 acc[9];
#define G1_STAGE(s, VM, LW, LX) do { \
    asm volatile("s_waitcnt vmcnt(" #VM ")\n\ts_barrier" : "+v"(xf[s][0]), "+v"(xf[s][1]) :: "memory"); \
    const lds_cptr pc = (lds_cptr)smem + (g & 3) * G1_SLOT + aoff, pn = (lds_cptr)smem + ((g + 1) & 3) * G1_SLOT + aoff; \
    SBAR(); \
    _Pragma("unroll") for (int i = 0; i < 18; ++i) { \
      acc[i % 9] = MFMA32(af[i % 6], __builtin_bit_cast(bf16x8, xf[s][i / 9]), acc[i % 9]); \
      SBAR(); \
      af[i % 6] = i < 12 ? G1_A(pc, i + 6) : G1_A(pn, i - 12); \
      if (LW && (i & 1) && i < 10) G1_WP((g + 3) & 3, i >> 1); \
      if (LX && i == 11) { if (g == 16) xsrc += 32 * 36864; G1_XP((s + 2) % 3, 0); } \
      if (LX && i == 13) G1_XP((s + 2) % 3, 1); \
      SBAR(); \
    } \
    ++g; } while (0)
  for (int un = 0; un < 2; ++un) {
#pragma unroll
    for (int b = 0; b < 9; ++b)
#pragma unroll
      for (int i = 0; i < 16; ++i) acc[b][i] = 0.f;
    for (int trip = un; trip < 6; ++trip) { G1_STAGE(0, 7, true, true); G1_STAGE(1, 7, true, true); G1_STAGE(2, 7, true, true); }
    if (un) { G1_STAGE(0, 7, false, true); G1_STAGE(1, 2, false, false); G1_STAGE(2, 0, false, false); }
    if (un == 0) STAMP_R(4); else STAMP_R(6);
    char* hp = ws + WS_H + ((size_t)(rt * 4 + un * 32 + w) * NBLK + ct * 9) * 1024 + lane * 16;
#pragma unroll
    for (int b = 0; b < 9; ++b) {
      u32x4 o;
#pragma unroll
      for (int i = 0; i < 4; ++i) o[i] = cvtpk(silu_mul(acc[b][2 * i], acc[b][8 + 2 * i]), silu_mul(acc[b][2 * i + 1], acc[b][9 + 2 * i]));
      *(u32x4*)(hp + b * 1024) = o;
    }
    if (un == 0) STAMP_R(5);
  }
  STAMP_FLUSH(K2MODE ? 4 : 1, 2, 6);
#undef G1_WP
#undef G1_XP
#undef G1_A
#undef G1_STAGE
}

template <int MODE>
__global__ __launch_bounds__(256, 2) void k2_attn_gemm1(const unsigned short* __restrict__ w1, const unsigned short* __restrict__ w3,
                                                        const unsigned short* __restrict__ w2, char* __restrict__ ws) {
  extern __shared__ __attribute__((aligned(16))) char smem[];
  const int lane = threadIdx.x & 63;
  const int w = __builtin_amdgcn_readfirstlane(threadIdx.x >> 6);
  const int u = blockIdx.x;
  STAMP_AT(1 + (MODE == 2) * 3, 0);
  if (MODE == 1) { attn_unit(smem, ws, u, w, lane); STAMP_AT(1, 1); return; }
  if (MODE == 2) { gemm1_unit(smem, ws, w1, w3, w2, u, w, lane); STAMP_AT(4, 1); return; }
  if (u < 256) {
#define GPRIO 1
    __builtin_amdgcn_s_setprio(GPRIO);
    gemm1_unit(smem, ws, w1, w3, w2, u, w, lane);
  } else {
    attn_unit(smem, ws, u - 256, w, lane);
  }
  STAMP_AT(1, 1);
}

constexpr int G2_STAGE = 18432, G2_NB = 8, G2_D = 6, K3_LDS = G2_NB * G2_STAGE;

__global__ __launch_bounds__(512, 2) void k3_gemm2(const unsigned short* __restrict__ w2, char* __restrict__ ws) {
  extern __shared__ __attribute__((aligned(16))) char smem[];
  const unsigned lds0 = (unsigned)(uintptr_t)smem;
  const int lane = threadIdx.x & 63;
  const int w = __builtin_amdgcn_readfirstlane(threadIdx.x >> 6);
  const int xcd = blockIdx.x & 7, j = blockIdx.x >> 3;
  const int sp = xcd >> 1, rt = (xcd & 1) * 8 + (j >> 2), ct = j & 3;
  const int st0 = sp == 0 ? 0 : 21 + (sp - 1) * 20, n = sp == 0 ? 21 : 20;
  const int wm = w & 3, kg = w >> 2, tb = rt * 4 + wm;
  const int c = lane & 15, q = lane >> 4;
  STAMP_AT(2, 0);
  unsigned woff[3];
#pragma unroll
  for (int i = 0; i < 3; ++i) {
    const unsigned ch = i < 2 ? (w * 2 + i) * 64 + lane : 1024 + w * 16 + (lane & 15);
    const unsigned row = ch / 18u;
    woff[i] = row * 1152u + (ch - row * 18u) * 16u + ct * 288;
  }
  const char* hsrc = ws + WS_H + (size_t)tb * NBLK * 1024 + (size_t)st0 * 4096;
  const unsigned hoff = (2 * kg + (q >> 1)) * 1024 + (q & 1) * 512 + c * 16;
  const char* wob = ws + WS_WO - (size_t)72 * 73728;
  const int rot = (j >> 2) * 5 / 2;
  int sw = rot, sh = rot;
  auto issue_w = [&](int i, int slot, int m) {
    const int st = st0 + sw;
    const char* base = (st < 72 ? (const char*)w2 : wob) + (size_t)st * 73728;
    glds16m(base, woff[i], lds0 + slot * G2_STAGE + (i < 2 ? (w * 2 + i) * 1024 : 16384 + w * 256), i < 2 ? m : (m & 0xFFFF), i < 2 ? m : 0);
  };
  f32x4 acc[2][9];
#pragma unroll
  for (int i = 0; i < 2; ++i)
#pragma unroll
    for (int b = 0; b < 9; ++b) acc[i][b] = f32x4{0.f, 0.f, 0.f, 0.f};
  u32x4 hf[3][2];
#pragma unroll
  for (int i = 0; i < 3; ++i) { hf[i][0] = u32x4{0, 0, 0, 0}; hf[i][1] = u32x4{0, 0, 0, 0}; }
#pragma unroll
  for (int s = 0; s < G2_D; ++s) {
#pragma unroll
    for (int i = 0; i < 3; ++i) issue_w(i, s, -1);
    sw = sw + 1 == n ? 0 : sw + 1;
    if (s >= 3) { gload2m<256>(hf[s - 3][0], hf[s - 3][1], hsrc + (size_t)sh * 4096, hoff, -1); sh = sh + 1 == n ? 0 : sh + 1; }
  }
  const int aoff = (32 * kg + 16 * (q >> 1) + 4 * (q & 1) + (c >> 2)) * 288 + 8 * (c & 3);
  bf16x8 af[9];
  asm volatile("s_waitcnt vmcnt(21)\n\ts_barrier" ::: "memory");
#pragma unroll
  for (int b = 0; b < 9; ++b) af[b] = cat(trread((lds_cptr)smem + aoff + b * 32), trread((lds_cptr)smem + aoff + 8 * 288 + b * 32));
  int t = 0;
#define G2_ITER(HS, BAR) do { \
    if (BAR) asm volatile("s_waitcnt vmcnt(10) lgkmcnt(0)\n\ts_barrier" : "+v"(hf[HS][0]), "+v"(hf[HS][1]) :: "memory"); \
    else asm volatile("s_waitcnt vmcnt(10)" : "+v"(hf[HS][0]), "+v"(hf[HS][1]) :: "memory"); \
    const int mw = negmask(t + G2_D - n), mh = negmask(t + 3 - n); \
    const lds_cptr nb = (lds_cptr)smem + ((t + 1) & 7) * G2_STAGE + aoff; \
    __builtin_amdgcn_sched_barrier(0); \
    _Pragma("unroll") for (int b = 0; b < 9; ++b) { \
      acc[0][b] = MFMA16(af[b], __builtin_bit_cast(bf16x8, hf[HS][0]), acc[0][b]); \
      acc[1][b] = MFMA16(af[b], __builtin_bit_cast(bf16x8, hf[HS][1]), acc[1][b]); \
      __builtin_amdgcn_sched_barrier(0); \
      af[b] = cat(trread(nb + b * 32), trread(nb + 8 * 288 + b * 32)); \
      if (b == 1 || b == 3 || b == 5) issue_w(b >> 1, (t + G2_D) & 7, mw); \
      __builtin_amdgcn_sched_barrier(0); \
    } \
    gload2m<256>(hf[HS][0], hf[HS][1], hsrc + (size_t)sh * 4096, hoff, mh); \
    sw = sw + 1 == n ? 0 : sw + 1; sh = sh + 1 == n ? 0 : sh + 1; \
    ++t; \
  } while (0)
  for (;;) {
    G2_ITER(0, true);
    G2_ITER(1, false);
    if (t == n) break;
    G2_ITER(2, false);
    if (t == n) break;
  }
#undef G2_ITER
  STAMP_AT(2, 1);
  asm volatile("s_waitcnt vmcnt(0) lgkmcnt(0)\n\ts_barrier" ::: "memory");
  f32x4* red = (f32x4*)(smem + (wm * 2) * 9216) + lane;
#pragma unroll
  for (int b = 0; b < 9; ++b) red[(kg ^ 1) * 576 + b * 64] = kg ? acc[0][b] : acc[1][b];
  BARRIER_LGKM();
  float* pp = (float*)(ws + WS_P) + ((size_t)sp * S_ + tb * 32 + kg * 16 + c) * D_ + ct * 144 + 4 * q;
#pragma unroll
  for (int b = 0; b < 9; ++b) *(f32x4*)(pp + b * 16) = (kg ? acc[1][b] : acc[0][b]) + red[kg * 576 + b * 64];
  STAMP_AT(2, 2);
}

__global__ __launch_bounds__(256) void k4_reduce(const unsigned short* __restrict__ x, const char* __restrict__ ws, unsigned short* __restrict__ out) {
  const size_t i = ((size_t)blockIdx.x * 256 + threadIdx.x) * 8;
  const float* p = (const float*)(ws + WS_P) + i;
  const u32x4 xv = *(const u32x4*)(x + i);
  f32x4 a = *(const f32x4*)p, b = *(const f32x4*)(p + 4);
#pragma unroll
  for (int s = 1; s < 4; ++s) { a += *(const f32x4*)(p + (size_t)s * S_ * D_); b += *(const f32x4*)(p + (size_t)s * S_ * D_ + 4); }
  *(u32x4*)(out + i) = u32x4{cvtpk(a[0] + bflo(xv[0]), a[1] + bfhi(xv[0])), cvtpk(a[2] + bflo(xv[1]), a[3] + bfhi(xv[1])),
                             cvtpk(b[0] + bflo(xv[2]), b[1] + bfhi(xv[2])), cvtpk(b[2] + bflo(xv[3]), b[3] + bfhi(xv[3]))};
}

extern "C" void kernel_launch(void* const* d_in, const int* in_sizes, int n_in, void* d_out, int out_size, void* d_ws, size_t ws_size,
                              hipStream_t stream) {
  const unsigned short* x = (const unsigned short*)d_in[0];
  const unsigned short* wq = (const unsigned short*)d_in[1];
  const unsigned short* wk = (const unsigned short*)d_in[2];
  const unsigned short* wv = (const unsigned short*)d_in[3];
  const unsigned short* wo = (const unsigned short*)d_in[4];
  const unsigned short* w1 = (const unsigned short*)d_in[6];
  const unsigned short* w3 = (const unsigned short*)d_in[7];
  const unsigned short* w2 = (const unsigned short*)d_in[8];
  char* ws = (char*)d_ws;
  (void)hipFuncSetAttribute((const void*)k1_ln_qkv, hipFuncAttributeMaxDynamicSharedMemorySize, K1_LDS);
  (void)hipFuncSetAttribute((const void*)k3_gemm2, hipFuncAttributeMaxDynamicSharedMemorySize, K3_LDS);
  k1_ln_qkv<<<256, 256, K1_LDS, stream>>>(x, wq, wk, wv, wo, ws);
  (void)hipFuncSetAttribute((const void*)k2_attn_gemm1<0>, hipFuncAttributeMaxDynamicSharedMemorySize, K2_LDS);
  k2_attn_gemm1<0><<<512, 256, K2_LDS, stream>>>(w1, w3, w2, ws);
  k3_gemm2<<<256, 512, K3_LDS, stream>>>(w2, ws);
  k4_reduce<<<S_ * D_ / 8 / 256, 256, 0, stream>>>(x, ws, (unsigned short*)d_out);
}
```

```cpp
#include <hip/hip_runtime.h>
#include <hip/hip_bf16.h>
#include <stdint.h>

typedef short bf16x8 __attribute__((ext_vector_type(8)));
typedef short bf16x4 __attribute__((ext_vector_type(4)));
typedef float f32x4 __attribute__((ext_vector_type(4)));
typedef float f32x16 __attribute__((ext_vector_type(16)));
typedef unsigned u32x4 __attribute__((ext_vector_type(4)));
typedef unsigned u32x2 __attribute__((ext_vector_type(2)));
typedef unsigned u32x4_a4 __attribute__((ext_vector_type(4), aligned(4)));
typedef __attribute__((address_space(3))) const char* lds_cptr;
typedef __attribute__((address_space(3))) char* lds_ptr;
#define DI __device__ __forceinline__

#define XCHG 0
#define K2MODE 0
constexpr int S_ = 2048, D_ = 576, F_ = 4608;
constexpr int NBLK = 324;
constexpr int KV_HS = 20224, KV_HSG = 20480;
constexpr int KV_K2 = 8192, KV_V = 10240;
constexpr size_t WS_XN = 0;
constexpr size_t WS_Q = WS_XN + (size_t)S_ * D_ * 2;
constexpr size_t WS_KV = WS_Q + (size_t)S_ * D_ * 2;
constexpr size_t WS_KMAX = WS_KV + 8 * KV_HSG;
constexpr size_t WS_WP = WS_KMAX + 1024;
constexpr size_t WS_H = WS_WP + (size_t)4 * 162 * 9216;
constexpr size_t WS_P = WS_H + (size_t)64 * NBLK * 1024;

DI unsigned cvtpk(float lo, float hi) {
  typedef __bf16 b2 __attribute__((ext_vector_type(2)));
  typedef float f2 __attribute__((ext_vector_type(2)));
  f2 v = {lo, hi};
  return __builtin_bit_cast(unsigned, __builtin_convertvector(v, b2));
}
DI float bflo(unsigned u) { return __uint_as_float(u << 16); }
DI float bfhi(unsigned u) { return __uint_as_float(u & 0xffff0000u); }
DI void glds16(const void* sbase, unsigned voff, unsigned lds_addr) {
  asm volatile("s_mov_b32 m0, %0\n\ts_nop 0\n\tglobal_load_lds_dwordx4 %1, %2" ::"s"(lds_addr), "v"(voff), "s"(sbase) : "memory");
}
DI void store16_wt(void* p, u32x4 v) {
  asm volatile("global_store_dwordx4 %0, %1, off sc1\n\ts_nop 1" ::"v"(p), "v"(v) : "memory");
}

DI bf16x4 trread(lds_cptr p) {
  return __builtin_amdgcn_ds_read_tr16_b64_v4i16((__attribute__((address_space(3))) bf16x4*)p);
}
DI bf16x8 cat(bf16x4 a, bf16x4 b) { return __builtin_shufflevector(a, b, 0, 1, 2, 3, 4, 5, 6, 7); }
#define MFMA32(a, b, c) __builtin_amdgcn_mfma_f32_32x32x16_bf16((a), (b), (c), 0, 0, 0)
#define MFMA32K8(a, b, c) __builtin_amdgcn_mfma_f32_32x32x8bf16_1k((a), (b), (c), 0, 0, 0)
#define MFMA16(a, b, c) __builtin_amdgcn_mfma_f32_16x16x32_bf16((a), (b), (c), 0, 0, 0)
#define MFMA16A(c, a, b) asm volatile("v_mfma_f32_16x16x32_bf16 %0, %1, %2, %0" : "+v"(c) : "v"(a), "v"(b))
#define MFMA_DRAIN() asm volatile("s_nop 7\n\ts_nop 7" ::: "memory")
#define SBAR() __builtin_amdgcn_sched_barrier(0)
#define PIN(x) asm volatile("" : "+v"(x))
#define LDS128(p) (*(const __attribute__((address_space(3))) bf16x8*)(p))
#define LDS64(p) (*(const __attribute__((address_space(3))) bf16x4*)(p))
#define WAIT_VM0() __builtin_amdgcn_s_waitcnt(0x0F70)
#define BARRIER_LGKM() asm volatile("s_waitcnt lgkmcnt(0)\n\ts_barrier" ::: "memory")
#define STAMP_AT(kid, slot)
#define STAMP_DECL
#define STAMP_R(slot)
#define STAMP_FLUSH(kid, lo, hi)

constexpr int K1_PK = 144 * 800, K1_XT = K1_PK + 3 * 9216, K1_LDS = K1_XT + 4 * 4352;
typedef float f32x2 __attribute__((ext_vector_type(2)));
DI float quad_sum(float v) {
  const auto a = __builtin_amdgcn_permlane32_swap(__float_as_uint(v), __float_as_uint(v), false, false);
  const float s = __uint_as_float(a[0]) + __uint_as_float(a[1]);
  const auto b = __builtin_amdgcn_permlane16_swap(__float_as_uint(s), __float_as_uint(s), false, false);
  return __uint_as_float(b[0]) + __uint_as_float(b[1]);
}
#define K1ROT 1
__global__ __launch_bounds__(512) void k1_ln_qkv(const unsigned short* __restrict__ x, const unsigned short* __restrict__ wq,
                                                 const unsigned short* __restrict__ wk, const unsigned short* __restrict__ wv,
                                                 const unsigned short* __restrict__ wo, const unsigned short* __restrict__ w2, char* __restrict__ ws) {
  extern __shared__ __attribute__((aligned(16))) char smem[];
  const int tid = threadIdx.x, lane = tid & 63;
  const int w = __builtin_amdgcn_readfirstlane(tid >> 6);
  const int g = (blockIdx.x >> 3) & 7, tg = (blockIdx.x & 7) * 4 + (blockIdx.x >> 6);
  const unsigned lds0 = (unsigned)(uintptr_t)smem;
  const int c = lane & 15, q = lane >> 4;
  STAMP_DECL;
  STAMP_R(0);
  if (w >= 4) {
    asm volatile("s_barrier" ::: "memory");
    if (g == 7) {
      for (int k = tid - 256; k < 576; k += 256) {
        const u32x4* a = (const u32x4*)(wq + (size_t)k * D_ + 560);
        const u32x4 a0 = a[0], a1 = a[1];
        const unsigned* kp = (const unsigned*)(wk + k * 18);
        const unsigned* vp = (const unsigned*)(wv + k * 18);
        const u32x4 k0 = *(const u32x4_a4*)kp, k1 = *(const u32x4_a4*)(kp + 4);
        const unsigned k2 = kp[8];
        const u32x4 v0 = *(const u32x4_a4*)vp, v1 = *(const u32x4_a4*)(vp + 4);
        const unsigned v2 = vp[8];
        char* d = smem + ((k >> 2) ^ ((k >> 3) & 1)) * 800 + (k & 3) * 192;
        *(u32x4*)(d) = a0; *(u32x4*)(d + 16) = a1;
        *(u32x4*)(d + 32) = k0; *(u32x4*)(d + 48) = k1; *(unsigned*)(d + 64) = k2;
        *(u32x4*)(d + 96) = v0; *(u32x4*)(d + 112) = v1; *(unsigned*)(d + 128) = v2;
      }
      BARRIER_LGKM();
      return;
    }
    if (lane < 48) {
      const unsigned row = lane / 12u, voff = row * 1152u + (lane - row * 12u) * 16u;
      const char* base = (const char*)wq + ((160 * g) & ~63) + ((w & 3) ^ ((w >> 1) & 1)) * 4608;
      int u = K1ROT * (tg & 3) * 9;
#pragma unroll 4
      for (int i = 0; i < 36; ++i) {
        glds16(base + u * 18432, voff, lds0 + (w & 3) * 800 + u * 3200);
        u = u == 35 ? 0 : u + 1;
      }
    }
    STAMP_R(4);
    const int job = (tg * 7 + g) * 3 + w - 5;
    if (w == 4 || job >= 648) {
      asm volatile("s_waitcnt vmcnt(0)\n\ts_barrier" ::: "memory");
      return;
    }
    const int j = job >> 2;
    const unsigned ra = j < 144 ? 16 : j < 160 ? 18 : 144, rb = j < 160 ? 2 : 18, rc = j < 144 ? 32 * j : j < 160 ? 36 * (j - 144) : 288 * (j - 160) + 16;
    const char* base = (const char*)(j < 144 ? w2 : wo) + (size_t)rc * 1152 + (job & 3) * 288;
#pragma unroll
    for (int v = 0; v < 9; ++v) {
      const unsigned ch = v * 64 + lane, row = ch / 18u, cc = ch - row * 18u;
      const unsigned src = ra * (row >> 4) + rb * ((row & 15) >> 1) + (row & 1);
      glds16(base, src * 1152u + cc * 16u, lds0 + K1_PK + (w - 5) * 9216 + v * 1024);
    }
    STAMP_R(5);
    asm volatile("s_waitcnt vmcnt(9)\n\ts_barrier\n\ts_waitcnt vmcnt(0)" ::: "memory");
    STAMP_R(2);
    const lds_cptr pk = (lds_cptr)smem + K1_PK + (w - 5) * 9216 + (16 * (q >> 1) + 4 * (q & 1) + (c >> 2)) * 288;
    char* wp = ws + WS_WP + (size_t)((job & 3) * 162 + j) * 9216 + lane * 16;
#pragma unroll
    for (int b = 0; b < 9; ++b) {
      const lds_cptr a = pk + (b < 8 ? 64 * (b >> 1) + 8 * (b & 1) + 16 * (c & 3) : 256 + 16 * (c & 1) + 8 * ((c >> 1) & 1));
      store16_wt(wp + b * 1024, __builtin_bit_cast(u32x4, cat(trread(a), trread(a + 8 * 288))));
    }
    STAMP_R(3);
    STAMP_FLUSH(0, 0, 7);
    return;
  }
  const int tok = tg * 64 + w * 16 + c;
  u32x4 xr[18];
  {
    const char* xp = (const char*)x + (tg * 64 + w * 16 + (c & 12)) * 1152 + q * 64 + (c & 3) * 16;
#pragma unroll
    for (int i = 0; i < 16; ++i) xr[i] = *(const u32x4*)(xp + (i & 3) * 1152 + (i >> 2) * 256);
#pragma unroll
    for (int i = 16; i < 18; ++i) xr[i] = *(const u32x4*)((const char*)x + tok * 1152 + i * 64 + q * 16);
    asm volatile("s_barrier" ::: "memory");
    STAMP_R(6);
    typedef __attribute__((address_space(3))) u32x4* lds_v4;
    const lds_ptr tw = (lds_ptr)smem + K1_XT + w * 4352 + (c & 12) * 272 + q * 64 + (c & 3) * 16, tr = (lds_ptr)smem + K1_XT + w * 4352 + c * 272 + q * 16;
#pragma unroll
    for (int gq = 0; gq < 4; ++gq) {
#pragma unroll
      for (int i = 0; i < 4; ++i) *(lds_v4)(tw + i * 272) = xr[4 * gq + i];
      asm volatile("" ::: "memory");
#pragma unroll
      for (int i = 0; i < 4; ++i) xr[4 * gq + i] = *(lds_v4)(tr + i * 64);
      asm volatile("" ::: "memory");
    }
    STAMP_R(4);
  }
  f32x2 d[72];
  f32x2 s2[2] = {{0.f, 0.f}, {0.f, 0.f}};
#pragma unroll
  for (int i = 0; i < 72; ++i) { d[i] = f32x2{bflo(xr[i >> 2][i & 3]), bfhi(xr[i >> 2][i & 3])}; s2[i & 1] += d[i]; }
  const float mean = quad_sum((s2[0][0] + s2[0][1]) + (s2[1][0] + s2[1][1])) * (1.f / 576.f);
  STAMP_R(5);
  f32x2 v2[2] = {{0.f, 0.f}, {0.f, 0.f}};
#pragma unroll
  for (int i = 0; i < 72; ++i) { d[i] -= mean; v2[i & 1] += d[i] * d[i]; }
  const float rstd = 1.f / sqrtf(quad_sum((v2[0][0] + v2[0][1]) + (v2[1][0] + v2[1][1])) * (1.f / 576.f) + 1e-5f);
#pragma unroll
  for (int i = 0; i < 72; ++i) { const f32x2 y = d[i] * rstd; xr[i >> 2][i & 3] = cvtpk(y[0], y[1]); }
  {
    char* xn = ws + WS_XN + (size_t)(tok >> 5) * 36864 + (w & 1) * 1024 + lane * 16;
#pragma unroll
    for (int i = 0; i < 18; ++i)
      if ((i & 7) == g) store16_wt(xn + i * 2048, xr[i]);
  }
  STAMP_R(1);
  asm volatile("s_barrier" ::: "memory");
  STAMP_R(2);
  f32x4 acc[5];
#pragma unroll
  for (int b = 0; b < 5; ++b) acc[b] = f32x4{0.f, 0.f, 0.f, 0.f};
  const int co = (c >> 2) * 192 + 8 * (c & 3) + (g < 7 ? (g & 1) * 32 : 0);
  const lds_cptr p0 = (lds_cptr)smem + (2 * q + (q & 1)) * 800 + co, p1 = (lds_cptr)smem + (2 * q + 1 - (q & 1)) * 800 + co;
  const lds_cptr p2 = p0 + 57600, p3 = p1 + 57600;
  bf16x4 al[10], ah[10];
#define K1_RD(n) do { al[(n) % 10] = trread(((n) < 45 ? p0 : p2) + ((n) % 45 / 5) * 6400 + ((n) % 5) * 32); \
                      ah[(n) % 10] = trread(((n) < 45 ? p1 : p3) + ((n) % 45 / 5) * 6400 + ((n) % 5) * 32); } while (0)
#pragma unroll
  for (int n = 0; n < 9; ++n) K1_RD(n);
  if (g < 7) {
#pragma unroll
    for (int n = 0; n < 90; ++n) {
      if (n + 9 < 90) K1_RD(n + 9);
      acc[n % 5] = MFMA16(cat(al[n % 10], ah[n % 10]), __builtin_bit_cast(bf16x8, xr[n / 5]), acc[n % 5]);
      SBAR();
    }
  } else {
#pragma unroll
    for (int n = 0; n < 90; ++n) {
      if (n + 9 < 90) K1_RD(n + 9);
      if (n % 5 < 3) acc[n % 5] = MFMA16(cat(al[n % 10], ah[n % 10]), __builtin_bit_cast(bf16x8, xr[n / 5]), acc[n % 5]);
      else acc[n % 5] = MFMA16(__builtin_bit_cast(bf16x8, xr[n / 5]), cat(al[n % 10], ah[n % 10]), acc[n % 5]);
      SBAR();
    }
  }
#undef K1_RD
  STAMP_R(7);
  constexpr float QSCALE = 0.23570226039551584f * 1.4426950408889634f;
  char* qp = ws + WS_Q + ((size_t)tok * D_ + 80 * g + 4 * q) * 2;
#define QST(b) *(u32x2*)(qp + (b) * 32) = u32x2{cvtpk(acc[b][0] * QSCALE, acc[b][1] * QSCALE), cvtpk(acc[b][2] * QSCALE, acc[b][3] * QSCALE)}
  QST(0);
  if (g < 7) {
    QST(1); QST(2); QST(3); QST(4);
  } else {
    char* kv = ws + WS_KV + (tok >> 8) * KV_HSG;
    const int kk = tok & 255;
    const u32x2 kA = {cvtpk(acc[1][0], acc[1][1]), cvtpk(acc[1][2], acc[1][3])};
    const unsigned kB = cvtpk(acc[2][0], acc[2][1]);
    *(u32x2*)(kv + (kk >> 5) * 1024 + ((q >> 1) * 32 + (kk & 31)) * 16 + (q & 1) * 8) = kA;
    if (q == 0) *(u32x2*)(kv + KV_K2 + kk * 8) = u32x2{kB, 0x3F803F80u};
    const int t0 = (tg * 64 + w * 16) & 255;
    char* vp = kv + KV_V + (t0 >> 4) * 624 + (q & 1) * 304 + (q >> 1) * 8;
    *(u32x2*)(vp + c * 16) = u32x2{cvtpk(acc[3][0], acc[3][1]), cvtpk(acc[3][2], acc[3][3])};
    if (c < 2) *(u32x2*)(vp + (16 + c) * 16) = u32x2{cvtpk(acc[4][0], acc[4][1]), cvtpk(acc[4][2], acc[4][3])};
    if (c == 2) *(u32x2*)(vp + 18 * 16) = u32x2{0x3F803F80u, 0x3F803F80u};
    if (c == 3 && (q & 1)) *(u32x2*)(vp + 19 * 16) = u32x2{0u, 0u};
    float n2 = bflo(kA[0]) * bflo(kA[0]) + bfhi(kA[0]) * bfhi(kA[0]) + bflo(kA[1]) * bflo(kA[1]) + bfhi(kA[1]) * bfhi(kA[1]);
    if (q == 0) n2 += bflo(kB) * bflo(kB) + bfhi(kB) * bfhi(kB);
    n2 = quad_sum(n2);
    n2 = fmaxf(n2, __shfl_xor(n2, 1)); n2 = fmaxf(n2, __shfl_xor(n2, 2));
    n2 = fmaxf(n2, __shfl_xor(n2, 4)); n2 = fmaxf(n2, __shfl_xor(n2, 8));
    if (lane == 0) ((float*)(ws + WS_KMAX))[tg * 4 + w] = n2;
  }
#undef QST
  STAMP_R(3);
  STAMP_FLUSH(0, 0, 7);
}

#define HSTORE(p, v) store16_wt(p, v)
constexpr int K2_FLAG = 4 * KV_HS;
constexpr int K2_LDS = K2_FLAG + 16;

#define KV_LOADS "s_mov_b32 m0, %0\n\ts_nop 0\n\t" \
               "global_load_lds_dwordx4 %1, %3\n\tglobal_load_lds_dwordx4 %1, %3 offset:1024\n\t" \
               "global_load_lds_dwordx4 %1, %3 offset:2048\n\tglobal_load_lds_dwordx4 %1, %3 offset:3072\n\t" \
               "s_add_u32 m0, %0, 0x1000\n\ts_mov_b32 exec_hi, %4\n\tglobal_load_lds_dwordx4 %2, %3\n\ts_mov_b32 exec_hi, -1\n"
DI void kv_issue(const char* src, unsigned va, unsigned vb, unsigned dst, int m5hi) {
  asm volatile(KV_LOADS ::"s"(dst), "v"(va), "v"(vb), "s"(src), "s"(m5hi) : "memory", "scc");
}
#define PFHS "9"
DI void kv_turn(int hs, const char* src, unsigned va, unsigned vb, unsigned dst, int m5hi, char* box, unsigned voff, u32x4 ones) {
  asm volatile("s_cmp_lt_u32 %5, 5\n\ts_cbranch_scc0 1f\n\ts_waitcnt vmcnt(5)\n\ts_barrier\n\t" KV_LOADS "\ts_branch 2f\n"
               "1:\n\ts_waitcnt vmcnt(0)\n\ts_barrier\n\ts_cmp_lg_u32 %5, " PFHS "\n\ts_cbranch_scc1 2f\n\t"
               "global_store_dwordx4 %7, %8, %6 offset:-3072 sc1\n\tglobal_store_dwordx4 %7, %8, %6 offset:-2048 sc1\n\t"
               "global_store_dwordx4 %7, %8, %6 offset:-1024 sc1\n\tglobal_store_dwordx4 %7, %8, %6 sc1\n\t"
               "global_store_dwordx4 %7, %8, %6 offset:1024 sc1\n\tglobal_store_dwordx4 %7, %8, %6 offset:2048 sc1\n\t"
               "global_store_dwordx4 %7, %8, %6 offset:3072 sc1\n"
               "2:"
               ::"s"(dst), "v"(va), "v"(vb), "s"(src), "s"(m5hi), "s"(hs), "s"(box), "v"(voff), "v"(ones) : "memory", "scc");
}
#undef KV_LOADS
DI bf16x4 q_tail(unsigned q2, float m, int h) {
  const float m1 = bflo(cvtpk(m, 0.f)), r1 = m - m1, m2 = bflo(cvtpk(r1, 0.f)), m3 = r1 - m2;
  return __builtin_bit_cast(bf16x4, h ? u32x2{0u, cvtpk(-m3, 0.f)} : u32x2{q2, cvtpk(-m1, -m2)});
}

DI void kv_first(char* smem, const char* kvg, int w, int lane) {
  const unsigned va = w * 5120 + lane * 16;
#pragma unroll
  for (int i = 0; i < 3; ++i) kv_issue(kvg + i * KV_HSG, va, va + 4096, (unsigned)(uintptr_t)smem + w * 5120 + i * KV_HS, w == 3 ? 0xFFFF : -1);
}

#define APRIO_HI 2
#define APRIO_LO 0
DI void attn_main(char* ws, char* smem, const char* kvg, char* box, int w, int lane, bf16x8 qa1, bf16x4 qa2, bf16x8 qb1, bf16x4 qb2, f32x16& oa, f32x16& ob) {
  const unsigned lds0 = (unsigned)(uintptr_t)smem;
  const int r = lane & 31, h = lane >> 5;
  const unsigned va = w * 5120 + lane * 16, vb = va + 4096, dst = lds0 + w * 5120;
  const int m5 = w == 3 ? 0xFFFF : -1;
  const int ok1 = lane * 16, ok2 = KV_K2 + r * 8, ov = KV_V + (r < 19 ? h * 304 + r * 16 : 608);
  const f32x16 zero = {};
  u32x4 ones = {~0u, ~0u, ~0u, ~0u};
  PIN(ones);
  STAMP_DECL;
  STAMP_R(2);
  asm volatile("s_waitcnt vmcnt(10)\n\ts_barrier" ::: "memory");
  STAMP_R(3);
  bf16x8 k1 = LDS128((lds_cptr)smem + ok1), v0 = LDS128((lds_cptr)smem + ov), v1 = {};
  bf16x4 k2 = LDS64((lds_cptr)smem + ok2);
  f32x16 sa = MFMA32(k1, qa1, MFMA32K8(k2, qa2, zero)), sb;
  u32x4 p0, p1 = {0u, 0u, 0u, 0u};
  float e2 = 0.f, e3 = 0.f;
#define SLOT(MF, RD, NEXT, S, i, PP, jp, P, j) do { \
    MF; SBAR(); __builtin_amdgcn_s_setprio(APRIO_LO); SBAR(); \
    float e0 = __builtin_amdgcn_exp2f(S[i]); PIN(e0); SBAR(); RD; SBAR(); \
    float e1 = __builtin_amdgcn_exp2f(S[i + 1]); PIN(e1); SBAR(); \
    PP[jp] = cvtpk(e2, e3); PIN(PP); SBAR(); \
    e2 = __builtin_amdgcn_exp2f(S[i + 2]); PIN(e2); SBAR(); PIN(NEXT); SBAR(); \
    e3 = __builtin_amdgcn_exp2f(S[i + 3]); PIN(e3); SBAR(); __builtin_amdgcn_s_setprio(APRIO_HI); SBAR(); \
    P[j] = cvtpk(e0, e1); PIN(P); SBAR(); } while (0)
#define PB(x) __builtin_bit_cast(bf16x8, x)
  for (int hs = 0; hs < 8; ++hs) {
    const lds_cptr cur = (lds_cptr)smem + (hs & 3) * KV_HS, nxt = (lds_cptr)smem + ((hs + 1) & 3) * KV_HS;
    const lds_cptr ck1 = cur + ok1, ck2 = cur + ok2, cv = cur + ov, nk1 = nxt + ok1, nk2 = nxt + ok2, nv = nxt + ov;
#pragma unroll
    for (int t = 0; t < 8; ++t) {
      if (t == 7) kv_turn(hs, kvg + ((hs + 3) & 7) * KV_HSG, va, vb, dst + ((hs + 3) & 3) * KV_HS, m5, box, ok1, ones);
      SBAR();
      SLOT(sb = MFMA32K8(k2, qb2, zero), k2 = t < 7 ? LDS64(ck2 + (t + 1) * 256) : LDS64(nk2), e0, sa, 0, p1, 3, p0, 0);
      SLOT(ob = MFMA32(v1, PB(p1), ob), v1 = LDS128(cv + (2 * t + 1) * 624), e0, sa, 4, p0, 1, p0, 2);
      SLOT(sb = MFMA32(k1, qb1, sb), k1 = t < 7 ? LDS128(ck1 + (t + 1) * 1024) : LDS128(nk1), v0, sa, 8, p0, 3, p1, 0);
      SLOT(oa = MFMA32(v0, PB(p0), oa), , k2, sa, 12, p1, 1, p1, 2);
      SLOT(sa = MFMA32K8(k2, qa2, zero), , v1, sb, 0, p1, 3, p0, 0);
      SLOT(oa = MFMA32(v1, PB(p1), oa), , k1, sb, 4, p0, 1, p0, 2);
      SLOT(sa = MFMA32(k1, qa1, sa), , e0, sb, 8, p0, 3, p1, 0);
      SLOT(ob = MFMA32(v0, PB(p0), ob), v0 = t < 7 ? LDS128(cv + (2 * t + 2) * 624) : LDS128(nv), e0, sb, 12, p1, 1, p1, 2);
    }
  }
#undef SLOT
  STAMP_R(4);
  STAMP_FLUSH(1, 2, 4);
  p1[3] = cvtpk(e2, e3);
  ob = MFMA32(v1, PB(p1), ob);
#undef PB
  asm volatile("s_waitcnt vmcnt(0) lgkmcnt(0)\n\ts_barrier" ::: "memory");
}

DI void attn_max(char* smem, const char* kvg, int w, int lane, bf16x8 qa1, bf16x4 qa2, bf16x8 qb1, bf16x4 qb2, float& ma, float& mb) {
  const unsigned lds0 = (unsigned)(uintptr_t)smem;
  const unsigned va = w * 5120 + lane * 16;
  const f32x16 zero = {};
  for (int hs = 0; hs < 8; ++hs) {
    kv_issue(kvg + hs * KV_HSG, va, va + 4096, lds0 + w * 5120, w == 3 ? 0xFFFF : -1);
    asm volatile("s_waitcnt vmcnt(0)\n\ts_barrier" ::: "memory");
#pragma unroll 1
    for (int t = 0; t < 8; ++t) {
      const bf16x8 k1 = LDS128((lds_cptr)smem + lane * 16 + t * 1024);
      const bf16x4 k2 = LDS64((lds_cptr)smem + KV_K2 + (lane & 31) * 8 + t * 256);
      const f32x16 sa = MFMA32(k1, qa1, MFMA32K8(k2, qa2, zero)), sb = MFMA32(k1, qb1, MFMA32K8(k2, qb2, zero));
#pragma unroll
      for (int i = 0; i < 16; ++i) { ma = fmaxf(ma, sa[i]); mb = fmaxf(mb, sb[i]); }
    }
    BARRIER_LGKM();
  }
  ma = fmaxf(ma, __shfl_xor(ma, 32));
  mb = fmaxf(mb, __shfl_xor(mb, 32));
}

DI void attn_store(char* hp, int hd, int lane, const f32x16& o, float l) {
  const float inv = 1.f / l;
  HSTORE(hp + (288 + hd) * 1024 + lane * 16,
         (u32x4{cvtpk(o[0] * inv, o[1] * inv), cvtpk(o[2] * inv, o[3] * inv), cvtpk(o[4] * inv, o[5] * inv), cvtpk(o[6] * inv, o[7] * inv)}));
  if (lane < 32) {
    const int p = 2 * (hd & 7);
    *(unsigned*)(hp + (320 + (hd >> 3)) * 1024 + (((p >> 2) & 1) * 32 + lane) * 16 + 2 * (4 * (p >> 3) + (p & 3))) = cvtpk(o[8] * inv, o[9] * inv);
  }
}

DI float half_sum(float v) {
  const auto t = __builtin_amdgcn_permlane32_swap(__float_as_uint(v), __float_as_uint(v), false, false);
  return __uint_as_float(t[0]) + __uint_as_float(t[1]);
}
DI void attn_unit(char* smem, char* ws, int u, int w, int lane) {
  const char* kvg = ws + WS_KV;
  kv_first(smem, kvg, w, lane);
  const int gw = u * 4 + w, hd = (gw & 15) * 2, tb = gw >> 4;
  const int r = lane & 31, h = lane >> 5;
  const char* qrow = ws + WS_Q + ((size_t)(tb * 32 + r) * D_ + 18 * hd) * 2;
  const u32x4 qa = *(const u32x4_a4*)(qrow + 16 * h), qb = *(const u32x4_a4*)(qrow + 36 + 16 * h);
  const unsigned qa2 = *(const unsigned*)(qrow + 32), qb2 = *(const unsigned*)(qrow + 68);
  const u32x2 kx = *(const u32x2*)(ws + WS_KMAX + lane * 8);
  unsigned ku = max(kx[0], kx[1]);
  ku = max(ku, (unsigned)__builtin_amdgcn_update_dpp(0, (int)ku, 0xB1, 0xF, 0xF, true));
  ku = max(ku, (unsigned)__builtin_amdgcn_update_dpp(0, (int)ku, 0x4E, 0xF, 0xF, true));
  ku = max(ku, (unsigned)__builtin_amdgcn_update_dpp(0, (int)ku, 0x141, 0xF, 0xF, true));
  ku = max(ku, (unsigned)__builtin_amdgcn_update_dpp(0, (int)ku, 0x140, 0xF, 0xF, true));
  const float km = __uint_as_float(max(max((unsigned)__builtin_amdgcn_readlane(ku, 0), (unsigned)__builtin_amdgcn_readlane(ku, 16)),
                                       max((unsigned)__builtin_amdgcn_readlane(ku, 32), (unsigned)__builtin_amdgcn_readlane(ku, 48))));
  float na = 0.f, nb = 0.f;
#pragma unroll
  for (int j = 0; j < 4; ++j) {
    na += bflo(qa[j]) * bflo(qa[j]) + bfhi(qa[j]) * bfhi(qa[j]);
    nb += bflo(qb[j]) * bflo(qb[j]) + bfhi(qb[j]) * bfhi(qb[j]);
  }
  na = half_sum(na) + bflo(qa2) * bflo(qa2) + bfhi(qa2) * bfhi(qa2);
  nb = half_sum(nb) + bflo(qb2) * bflo(qb2) + bfhi(qb2) * bfhi(qb2);
  float ma = sqrtf(na * km) * 1.001f, mb = sqrtf(nb * km) * 1.001f;
  volatile __attribute__((address_space(3))) int* flag = (volatile __attribute__((address_space(3))) int*)((lds_ptr)smem + K2_FLAG);
  if (w == 0 && lane == 0) *flag = 0;
  const bf16x8 qa1 = __builtin_bit_cast(bf16x8, qa), qb1 = __builtin_bit_cast(bf16x8, qb);
  f32x16 oa, ob;
  float la, lb;
  for (int attempt = 0;; ++attempt) {
#pragma unroll
    for (int i = 0; i < 16; ++i) { oa[i] = 0.f; ob[i] = 0.f; }
    attn_main(ws, smem, kvg, ws + WS_P + (size_t)gw * 7168 + 3072, w, lane, qa1, q_tail(qa2, ma, h), qb1, q_tail(qb2, mb, h), oa, ob);
    la = __shfl(oa[10], r); lb = __shfl(ob[10], r);
    if (attempt) break;
    if (!(la >= 8.2718061e-25f && lb >= 8.2718061e-25f)) *flag = 1;
    BARRIER_LGKM();
    const int bad = *flag;
    if (!__builtin_amdgcn_readfirstlane(bad)) break;
    ma = -INFINITY; mb = -INFINITY;
    attn_max(smem, kvg, w, lane, qa1, q_tail(qa2, 0.f, h), qb1, q_tail(qb2, 0.f, h), ma, mb);
    kv_first(smem, kvg, w, lane);
  }
  char* hp = ws + WS_H + (size_t)tb * NBLK * 1024;
  attn_store(hp, hd, lane, oa, la);
  attn_store(hp, hd + 1, lane, ob, lb);
}

DI float silu_mul(float g, float u) {
  const float e = __builtin_amdgcn_exp2f(g * -1.4426950408889634f);
  return g * __builtin_amdgcn_rcpf(1.f + e) * u;
}

constexpr int G1_W3 = 9216, G1_SLOT = G1_W3 + 9216;
DI void glds16h(const void* sbase, unsigned voff, unsigned lds_addr) {
  asm volatile("s_mov_b32 m0, %0\n\ts_mov_b32 exec_hi, 0\n\tglobal_load_lds_dwordx4 %1, %2\n\ts_mov_b32 exec_hi, -1" ::"s"(lds_addr), "v"(voff), "s"(sbase) : "memory");
}
template <int OFF>
DI void gload16(u32x4& a, const void* sbase, unsigned voff) {
  asm volatile("global_load_dwordx4 %0, %1, %2 offset:%3" : "+v"(a) : "v"(voff), "s"(sbase), "i"(OFF) : "memory");
}

DI void gemm1_unit(char* smem, char* ws, const unsigned short* w1, const unsigned short* w3, const unsigned short* w2, int u, int w, int lane) {
  const unsigned lds0 = (unsigned)(uintptr_t)smem;
  const int j = u >> 3, ct = (u & 7) * 4 + (j & 3), rt = j >> 2;
  const char* wsrc = (const char*)((w >> 1) ? w3 : w1) + (w & 1) * 16 * 9216 + ct * 288;
  const unsigned wdst = lds0 + (w >> 1) * G1_W3 + (w & 1) * 4608;
  unsigned woff[5];
#pragma unroll
  for (int i = 0; i < 5; ++i) { const unsigned ch = i * 64 + (i < 4 ? lane : lane & 31), row = ch / 18u; woff[i] = (row ^ ((row >> 1) & 4u)) * 9216u + (ch - row * 18u) * 16u; }
  const char* xsrc = ws + WS_XN + (size_t)(rt * 4 + w) * 36864;
  const unsigned xoff = lane * 16;
  int sw = rt * 9 / 4, sx = sw;
  u32x4 xf[3][2];
#pragma unroll
  for (int i = 0; i < 3; ++i) { xf[i][0] = u32x4{0, 0, 0, 0}; xf[i][1] = u32x4{0, 0, 0, 0}; }
#define G1_WP(slot, i) do { if ((i) < 4) glds16(wsrc + (size_t)sw * 294912, woff[i], wdst + (slot) * G1_SLOT + (i) * 1024); \
    else { glds16h(wsrc + (size_t)sw * 294912, woff[4], wdst + (slot) * G1_SLOT + 4096); sw = sw == 17 ? 0 : sw + 1; } } while (0)
#define G1_XP(set, i) do { if ((i) == 0) gload16<0>(xf[set][0], xsrc + sx * 2048, xoff); \
    else { gload16<1024>(xf[set][1], xsrc + sx * 2048, xoff); sx = sx == 17 ? 0 : sx + 1; } } while (0)
#pragma unroll
  for (int s = 0; s < 3; ++s) {
#pragma unroll
    for (int i = 0; i < 5; ++i) G1_WP(s, i);
    if (s) { G1_XP(s - 1, 0); G1_XP(s - 1, 1); }
  }
  const int c = lane & 15, q = lane >> 4;
  const int aoff0 = (8 * q + 4 * (q & 1) + (c >> 2)) * 288 + 8 * (c & 3), aoff1 = (8 * q + 4 * ((q & 1) ^ 1) + (c >> 2)) * 288 + 8 * (c & 3);
#define G1_RD(p, i) trread((p) + ((i) / 9) * G1_W3 + ((i) % 9) * 32)
  bf16x4 al[6], ah[6];
  STAMP_DECL;
  STAMP_R(2);
  asm volatile("s_waitcnt vmcnt(14)\n\ts_barrier" ::: "memory");
  STAMP_R(3);
#pragma unroll
  for (int i = 0; i < 6; ++i) { al[i] = G1_RD((lds_cptr)smem + aoff0, i); if (i < 5) ah[i] = G1_RD((lds_cptr)smem + aoff1, i); }
  int g = 0;
  f32x4 acc[36];
#define G1_STAGE(s, VM, LW, LX) do { \
    asm volatile("s_waitcnt vmcnt(" #VM ")\n\ts_barrier" : "+v"(xf[s][0]), "+v"(xf[s][1]) :: "memory"); \
    const lds_cptr pc = (lds_cptr)smem + (g & 3) * G1_SLOT, pn = (lds_cptr)smem + ((g + 1) & 3) * G1_SLOT; \
    const lds_cptr pc0 = pc + aoff0, pc1 = pc + aoff1, pn0 = pn + aoff0, pn1 = pn + aoff1; \
    SBAR(); \
    _Pragma("unroll") for (int i = 0; i < 18; ++i) { \
      MFMA16A(acc[2 * i], cat(al[i % 6], ah[i % 6]), xf[s][0]); \
      SBAR(); \
      ah[(i + 5) % 6] = i < 13 ? G1_RD(pc1, i + 5) : G1_RD(pn1, i - 13); \
      if (LW && (i & 1) && i < 10) G1_WP((g + 3) & 3, i >> 1); \
      if (LX && i == 11) { if (g == 16) xsrc += 32 * 36864; G1_XP((s + 2) % 3, 0); } \
      if (LX && i == 13) G1_XP((s + 2) % 3, 1); \
      SBAR(); \
      MFMA16A(acc[2 * i + 1], cat(al[i % 6], ah[i % 6]), xf[s][1]); \
      SBAR(); \
      al[i % 6] = i < 12 ? G1_RD(pc0, i + 6) : G1_RD(pn0, i - 12); \
      SBAR(); \
    } \
    ++g; } while (0)
  for (int un = 0; un < 2; ++un) {
#pragma unroll
    for (int b = 0; b < 36; ++b) acc[b] = f32x4{0.f, 0.f, 0.f, 0.f};
    for (int trip = un; trip < 6; ++trip) { G1_STAGE(0, 7, true, true); G1_STAGE(1, 7, true, true); G1_STAGE(2, 7, true, true); }
    if (un) { G1_STAGE(0, 7, false, true); G1_STAGE(1, 2, false, false); G1_STAGE(2, 0, false, false); }
    MFMA_DRAIN();
    if (un == 0) STAMP_R(4); else STAMP_R(6);
    char* hp = ws + WS_H + ((size_t)(rt * 4 + un * 32 + w) * NBLK + ct * 9) * 1024 + ((q & 1) * 32 + (q >> 1) * 16 + c) * 16;
#pragma unroll
    for (int b = 0; b < 9; ++b) {
      PIN(acc[2 * b]); PIN(acc[2 * b + 1]); PIN(acc[18 + 2 * b]); PIN(acc[19 + 2 * b]);
      unsigned t[2][2];
#pragma unroll
      for (int i = 0; i < 4; ++i) {
        const f32x4 &ga = acc[2 * b + (i >> 1)], &ua = acc[18 + 2 * b + (i >> 1)];
        t[i >> 1][i & 1] = cvtpk(silu_mul(ga[2 * (i & 1)], ua[2 * (i & 1)]), silu_mul(ga[2 * (i & 1) + 1], ua[2 * (i & 1) + 1]));
      }
      const auto s0 = __builtin_amdgcn_permlane32_swap(t[0][0], t[1][0], false, false), s1 = __builtin_amdgcn_permlane32_swap(t[0][1], t[1][1], false, false);
      HSTORE(hp + b * 1024, (u32x4{s0[0], s1[0], s0[1], s1[1]}));
    }
    if (un == 0) STAMP_R(5);
  }
  STAMP_FLUSH(K2MODE ? 4 : 1, 2, 6);
#undef G1_WP
#undef G1_XP
#undef G1_RD
#undef G1_STAGE
}

template <int MODE>
__global__ __launch_bounds__(256, 2) void k2_attn_gemm1(const unsigned short* __restrict__ w1, const unsigned short* __restrict__ w3,
                                                        const unsigned short* __restrict__ w2, char* __restrict__ ws) {
  extern __shared__ __attribute__((aligned(16))) char smem[];
  const int lane = threadIdx.x & 63;
  const int w = __builtin_amdgcn_readfirstlane(threadIdx.x >> 6);
  const int u = blockIdx.x;
  STAMP_AT(1 + (MODE == 2) * 3, 0);
  if (MODE == 1) { attn_unit(smem, ws, u, w, lane); STAMP_AT(1, 1); return; }
  if (MODE == 2) { gemm1_unit(smem, ws, w1, w3, w2, u, w, lane); STAMP_AT(4, 1); return; }
  if (u < 256) {
#define GPRIO 1
    __builtin_amdgcn_s_setprio(GPRIO);
    gemm1_unit(smem, ws, w1, w3, w2, u, w, lane);
  } else {
    attn_unit(smem, ws, u - 256, w, lane);
  }
  STAMP_AT(1, 1);
}

#define PSTORE(p, v) store16_wt(p, v)
#define PSLEEP 1
constexpr int G2_STAGE = 18432, K3_LDS = 8 * G2_STAGE;
template <int OFF>
DI void bload16(u32x4& a, __amdgpu_buffer_rsrc_t r, unsigned voff, unsigned soff) {
  asm volatile("buffer_load_dwordx4 %0, %1, %2, %3 offen offset:%4" : "=v"(a) : "v"(voff), "s"(r), "s"(soff), "i"(OFF) : "memory");
}
template <int OFF>
DI void blds16(__amdgpu_buffer_rsrc_t r, unsigned voff, unsigned soff) {
  asm volatile("buffer_load_dwordx4 %0, %1, %2 offen offset:%3 lds" ::"v"(voff), "s"(r), "s"(soff), "i"(OFF) : "memory");
}
template <int OFF>
DI void blds4(__amdgpu_buffer_rsrc_t r, unsigned voff, unsigned soff) {
  asm volatile("buffer_load_dword %0, %1, %2 offen offset:%3 lds" ::"v"(voff), "s"(r), "s"(soff), "i"(OFF) : "memory");
}
template <int ADD>
DI void set_m0(unsigned base) { asm volatile("s_add_u32 m0, %0, %1" ::"s"(base), "i"(ADD) : "memory", "scc"); }
template <int N>
DI void wait_bar(u32x4& a, u32x4& b) { asm volatile("s_waitcnt vmcnt(%2)\n\ts_barrier" : "+v"(a), "+v"(b) : "i"(N) : "memory"); }

template <int T>
DI void g2_iter(f32x4 (&acc)[2][9], bf16x8 (&af)[9], u32x4 (&hf)[6][2], const lds_cptr (&ap)[3], __amdgpu_buffer_rsrc_t rw, __amdgpu_buffer_rsrc_t rh,
                unsigned w16, unsigned w4, unsigned hoff, unsigned hoff1, unsigned wdst) {
  constexpr int X = T + 1, NH = (T + 4 < 20 ? T + 4 : 20) - X + 1, NW = (T + 5 < 20 ? T + 5 : 20) - X;
  wait_bar<(T == 20 ? 0 : 2 * NH + 3 * NW)>(hf[T % 6][0], hf[T % 6][1]);
  SBAR();
#pragma unroll
  for (int b = 0; b < 9; ++b) {
    MFMA16A(acc[0][b], af[b], hf[T % 6][0]);
    SBAR();
    if (T + 5 <= 20 && b == 0) bload16<0>(hf[(T + 5) % 6][0], rh, hoff, (T + 5) * 4096);
    if (T + 5 <= 20 && b == 1) bload16<0>(hf[(T + 5) % 6][1], rh, hoff1, (T + 5) * 4096);
    if (T + 6 <= 20 && b == 2) set_m0<((T + 6) & 7) * G2_STAGE>(wdst);
    if (T + 6 <= 20 && b == 3) blds16<0>(rw, w16, (T + 6) * G2_STAGE);
    if (T + 6 <= 20 && b == 5) blds16<1024>(rw, w16, (T + 6) * G2_STAGE);
    if (T + 6 <= 20 && b == 7) blds4<2048>(rw, w4, (T + 6) * G2_STAGE);
    SBAR();
    MFMA16A(acc[1][b], af[b], hf[T % 6][1]);
    SBAR();
    if (T < 20) af[b] = LDS128(ap[((T + 1) & 7) / 3] + (((T + 1) & 7) % 3) * G2_STAGE + b * 1024);
    SBAR();
  }
}
template <int T, int E, typename... A>
DI void g2_run(A&... a) { if constexpr (T < E) { g2_iter<T>(a...); g2_run<T + 1, E>(a...); } }

__global__ __launch_bounds__(512, 2) void k3_gemm2(const unsigned short* __restrict__ x, char* __restrict__ ws, unsigned short* __restrict__ out) {
  extern __shared__ __attribute__((aligned(16))) char smem[];
  const unsigned lds0 = (unsigned)(uintptr_t)smem;
  const int lane = threadIdx.x & 63;
  const int w = __builtin_amdgcn_readfirstlane(threadIdx.x >> 6);
  const int xcd = blockIdx.x & 7, j = blockIdx.x >> 3;
  const int sp = xcd >> 1, rt = (xcd & 1) * 8 + (j >> 2), ct = j & 3;
  const int wm = w & 3, kg = w >> 2;
  const int c = lane & 15, q = lane >> 4;
  STAMP_DECL;
  STAMP_R(0);
  const __amdgpu_buffer_rsrc_t rw = __builtin_amdgcn_make_buffer_rsrc(ws + WS_WP + (size_t)(ct * 81 + sp * 20) * G2_STAGE + w * 2304, 0, 21 * G2_STAGE, 0x00020000);
  const __amdgpu_buffer_rsrc_t rh = __builtin_amdgcn_make_buffer_rsrc(ws + WS_H + (size_t)(rt * 4 + wm) * NBLK * 1024 + sp * 81920 + kg * 2048, 0, 21 * 4096, 0x00020000);
  unsigned w16 = lane * 16, w4 = lane * 4, wdst = lds0 + w * 2304;
  unsigned hoff = (q >> 1) * 1024 + (q & 1) * 512 + c * 16 + kg * 256, hoff1 = hoff ^ 256;
  u32x4 hf[6][2];
#define G2_PW(s) do { set_m0<(s) * G2_STAGE>(wdst); asm volatile("s_nop 0"); blds16<0>(rw, w16, (s) * G2_STAGE); blds16<1024>(rw, w16, (s) * G2_STAGE); blds4<2048>(rw, w4, (s) * G2_STAGE); } while (0)
#define G2_PH(s) do { bload16<0>(hf[s][0], rh, hoff, (s) * 4096); bload16<0>(hf[s][1], rh, hoff1, (s) * 4096); } while (0)
#define G2_PB() asm volatile("s_barrier" ::: "memory")
  G2_PW(0); G2_PH(0); G2_PB(); G2_PW(1); G2_PH(1); G2_PB(); G2_PW(2); G2_PH(2); G2_PB(); G2_PW(3); G2_PH(3); G2_PB(); G2_PW(4); G2_PH(4); G2_PB(); G2_PW(5);
#undef G2_PB
#undef G2_PW
#undef G2_PH
  f32x4 acc[2][9];
#pragma unroll
  for (int i = 0; i < 2; ++i)
#pragma unroll
    for (int b = 0; b < 9; ++b) { acc[i][b] = f32x4{0.f, 0.f, 0.f, 0.f}; PIN(acc[i][b]); }
  lds_cptr ap[3];
#pragma unroll
  for (int i = 0; i < 3; ++i) { ap[i] = (lds_cptr)smem + i * 3 * G2_STAGE + kg * 9216 + lane * 16; PIN(ap[i]); }
  bf16x8 af[9];
  asm volatile("s_waitcnt vmcnt(25)\n\ts_barrier" ::: "memory");
  STAMP_R(4);
#pragma unroll
  for (int b = 0; b < 9; ++b) af[b] = LDS128(ap[0] + b * 1024);
  g2_run<0, 6>(acc, af, hf, ap, rw, rh, w16, w4, hoff, hoff1, wdst);
  g2_run<6, 14>(acc, af, hf, ap, rw, rh, w16, w4, hoff, hoff1, wdst);
  g2_run<14, 20>(acc, af, hf, ap, rw, rh, w16, w4, hoff, hoff1, wdst);
  if (sp == 3) g2_iter<20>(acc, af, hf, ap, rw, rh, w16, w4, hoff, hoff1, wdst);
  STAMP_R(1);
  asm volatile("s_nop 7\n\ts_waitcnt vmcnt(0) lgkmcnt(0)\n\ts_barrier" : "+v"(hf[20 % 6][0]), "+v"(hf[20 % 6][1]) :: "memory");
#pragma unroll
  for (int b = 0; b < 9; ++b) { PIN(acc[0][b]); PIN(acc[1][b]); }
  f32x4* red = (f32x4*)(smem + (wm * 2) * 9216) + lane;
#pragma unroll
  for (int b = 0; b < 9; ++b) red[(kg ^ 1) * 576 + b * 64] = acc[1][b];
  BARRIER_LGKM();
  STAMP_R(7);
  f32x4 v[10];
#pragma unroll
  for (int b = 0; b < 9; ++b) v[b] = acc[0][b] + red[kg * 576 + b * 64];
#pragma unroll
  for (int i = 0; i < 4; ++i) v[9][i] = __uint_as_float(__builtin_amdgcn_permlane32_swap(__float_as_uint(v[8][i]), __float_as_uint(v[8][i]), false, false)[1]);
  char* box = ws + WS_P + (size_t)(((rt * 4 + ct) * 4 + sp) * 8 + w) * 4608 + lane * 16;
#define PK8(a, b) u32x4{cvtpk(a[0], a[1]), cvtpk(a[2], a[3]), cvtpk(b[0], b[1]), cvtpk(b[2], b[3])}
#pragma unroll
  for (int j = 0; j < 4; ++j) PSTORE(box + j * 1024, PK8(v[2 * j], v[2 * j + 1]));
  if (lane < 32) PSTORE(box + 4096, PK8(v[8], v[9]));
#undef PK8
  STAMP_R(2);
  STAMP_FLUSH(2, 0, 7);
}
__global__ __launch_bounds__(512) void k4_sum(const unsigned short* __restrict__ x, const char* __restrict__ ws, unsigned short* __restrict__ out) {
  const int lane = threadIdx.x & 63;
  const int w = __builtin_amdgcn_readfirstlane(threadIdx.x >> 6);
  const int tile = blockIdx.x >> 2, j = blockIdx.x & 3, rt = tile >> 2, ct = tile & 3, wm = w & 3, kg = w >> 2;
  const size_t orow = (size_t)(rt * 128 + wm * 32 + kg * 16) * D_ + ct * 144;
  const char* box = ws + WS_P + (size_t)(tile * 32 + w) * 4608;
  const bool tail = wm == j;
  const int src = (lane & 3) * 16 + (lane >> 2), srct = (lane & 1) * 16 + ((lane & 31) >> 1);
  const size_t oi = orow + (lane >> 2) * D_ + j * 32 + (lane & 3) * 8, ot = orow + ((lane & 31) >> 1) * D_ + 128 + (lane & 1) * 8;
  u32x4 r[4], t[4], xt;
#pragma unroll
  for (int i = 0; i < 4; ++i) r[i] = *(const u32x4*)(box + i * 36864 + j * 1024 + lane * 16);
  const u32x4 xv = *(const u32x4*)(x + oi);
  if (tail) {
#pragma unroll
    for (int i = 0; i < 4; ++i) t[i] = *(const u32x4*)(box + i * 36864 + 4096 + (lane & 31) * 16);
    xt = *(const u32x4*)(x + ot);
  }
#define FIN(o, xx, r, src) _Pragma("unroll") for (int k = 0; k < 4; ++k) { \
    const float e = (bflo(r[0][k]) + bflo(r[1][k])) + (bflo(r[2][k]) + bflo(r[3][k])), f = (bfhi(r[0][k]) + bfhi(r[1][k])) + (bfhi(r[2][k]) + bfhi(r[3][k])); \
    o[k] = cvtpk(bflo(xx[k]) + __shfl(e, src), bfhi(xx[k]) + __shfl(f, src)); }
  u32x4 o;
  FIN(o, xv, r, src);
  *(u32x4*)(out + oi) = o;
  if (tail) {
    FIN(o, xt, t, srct);
    if (lane < 32) *(u32x4*)(out + ot) = o;
  }
#undef FIN
}

extern "C" void kernel_launch(void* const* d_in, const int* in_sizes, int n_in, void* d_out, int out_size, void* d_ws, size_t ws_size,
                              hipStream_t stream) {
  const unsigned short* x = (const unsigned short*)d_in[0];
  const unsigned short* wq = (const unsigned short*)d_in[1];
  const unsigned short* wk = (const unsigned short*)d_in[2];
  const unsigned short* wv = (const unsigned short*)d_in[3];
  const unsigned short* wo = (const unsigned short*)d_in[4];
  const unsigned short* w1 = (const unsigned short*)d_in[6];
  const unsigned short* w3 = (const unsigned short*)d_in[7];
  const unsigned short* w2 = (const unsigned short*)d_in[8];
  char* ws = (char*)d_ws;
  (void)hipFuncSetAttribute((const void*)k1_ln_qkv, hipFuncAttributeMaxDynamicSharedMemorySize, K1_LDS);
  (void)hipFuncSetAttribute((const void*)k3_gemm2, hipFuncAttributeMaxDynamicSharedMemorySize, K3_LDS);
  k1_ln_qkv<<<256, 512, K1_LDS, stream>>>(x, wq, wk, wv, wo, w2, ws);
  (void)hipFuncSetAttribute((const void*)k2_attn_gemm1<0>, hipFuncAttributeMaxDynamicSharedMemorySize, K2_LDS);
  k2_attn_gemm1<0><<<512, 256, K2_LDS, stream>>>(w1, w3, w2, ws);
  k3_gemm2<<<256, 512, K3_LDS, stream>>>(x, ws, (unsigned short*)d_out);
  k4_sum<<<256, 512, 0, stream>>>(x, ws, (unsigned short*)d_out);
}
```

```cpp
#include <hip/hip_runtime.h>
#include <hip/hip_bf16.h>
#include <stdint.h>

typedef short bf16x8 __attribute__((ext_vector_type(8)));
typedef short bf16x4 __attribute__((ext_vector_type(4)));
typedef float f32x4 __attribute__((ext_vector_type(4)));
typedef float f32x16 __attribute__((ext_vector_type(16)));
typedef unsigned u32x4 __attribute__((ext_vector_type(4)));
typedef unsigned u32x2 __attribute__((ext_vector_type(2)));
typedef unsigned u32x4_a4 __attribute__((ext_vector_type(4), aligned(4)));
typedef __attribute__((address_space(3))) const char* lds_cptr;
typedef __attribute__((address_space(3))) char* lds_ptr;
#define DI __device__ __forceinline__

#define XCHG 1
#define K2MODE 0
constexpr int S_ = 2048, D_ = 576, F_ = 4608;
constexpr int NBLK = 324;
constexpr int KV_HS = 20224, KV_HSG = 20480;
constexpr int KV_K2 = 8192, KV_V = 10240;
constexpr size_t WS_XN = 0;
constexpr size_t WS_Q = WS_XN + (size_t)S_ * D_ * 2;
constexpr size_t WS_KV = WS_Q + (size_t)S_ * D_ * 2;
constexpr size_t WS_KMAX = WS_KV + 8 * KV_HSG;
constexpr size_t WS_WP = WS_KMAX + 1024;
constexpr size_t WS_H = WS_WP + (size_t)4 * 162 * 9216;
constexpr size_t WS_P = WS_H + (size_t)64 * NBLK * 1024;

DI unsigned cvtpk(float lo, float hi) {
  typedef __bf16 b2 __attribute__((ext_vector_type(2)));
  typedef float f2 __attribute__((ext_vector_type(2)));
  f2 v = {lo, hi};
  return __builtin_bit_cast(unsigned, __builtin_convertvector(v, b2));
}
DI float bflo(unsigned u) { return __uint_as_float(u << 16); }
DI float bfhi(unsigned u) { return __uint_as_float(u & 0xffff0000u); }
DI void glds16(const void* sbase, unsigned voff, unsigned lds_addr) {
  asm volatile("s_mov_b32 m0, %0\n\ts_nop 0\n\tglobal_load_lds_dwordx4 %1, %2" ::"s"(lds_addr), "v"(voff), "s"(sbase) : "memory");
}
DI void store16_wt(void* p, u32x4 v) {
  asm volatile("global_store_dwordx4 %0, %1, off sc1\n\ts_nop 1" ::"v"(p), "v"(v) : "memory");
}

DI bf16x4 trread(lds_cptr p) {
  return __builtin_amdgcn_ds_read_tr16_b64_v4i16((__attribute__((address_space(3))) bf16x4*)p);
}
DI bf16x8 cat(bf16x4 a, bf16x4 b) { return __builtin_shufflevector(a, b, 0, 1, 2, 3, 4, 5, 6, 7); }
#define MFMA32(a, b, c) __builtin_amdgcn_mfma_f32_32x32x16_bf16((a), (b), (c), 0, 0, 0)
#define MFMA32K8(a, b, c) __builtin_amdgcn_mfma_f32_32x32x8bf16_1k((a), (b), (c), 0, 0, 0)
#define MFMA16(a, b, c) __builtin_amdgcn_mfma_f32_16x16x32_bf16((a), (b), (c), 0, 0, 0)
#define MFMA16A(c, a, b) asm volatile("v_mfma_f32_16x16x32_bf16 %0, %1, %2, %0" : "+v"(c) : "v"(a), "v"(b))
#define MFMA_DRAIN() asm volatile("s_nop 7\n\ts_nop 7" ::: "memory")
#define SBAR() __builtin_amdgcn_sched_barrier(0)
#define PIN(x) asm volatile("" : "+v"(x))
#define LDS128(p) (*(const __attribute__((address_space(3))) bf16x8*)(p))
#define LDS64(p) (*(const __attribute__((address_space(3))) bf16x4*)(p))
#define WAIT_VM0() __builtin_amdgcn_s_waitcnt(0x0F70)
#define BARRIER_LGKM() asm volatile("s_waitcnt lgkmcnt(0)\n\ts_barrier" ::: "memory")
#define STAMP_AT(kid, slot)
#define STAMP_DECL
#define STAMP_R(slot)
#define STAMP_FLUSH(kid, lo, hi)

constexpr int K1_PK = 144 * 800, K1_XT = K1_PK + 3 * 9216, K1_LDS = K1_XT + 4 * 4352;
typedef float f32x2 __attribute__((ext_vector_type(2)));
DI float quad_sum(float v) {
  const auto a = __builtin_amdgcn_permlane32_swap(__float_as_uint(v), __float_as_uint(v), false, false);
  const float s = __uint_as_float(a[0]) + __uint_as_float(a[1]);
  const auto b = __builtin_amdgcn_permlane16_swap(__float_as_uint(s), __float_as_uint(s), false, false);
  return __uint_as_float(b[0]) + __uint_as_float(b[1]);
}
#define K1ROT 1
__global__ __launch_bounds__(512) void k1_ln_qkv(const unsigned short* __restrict__ x, const unsigned short* __restrict__ wq,
                                                 const unsigned short* __restrict__ wk, const unsigned short* __restrict__ wv,
                                                 const unsigned short* __restrict__ wo, const unsigned short* __restrict__ w2, char* __restrict__ ws) {
  extern __shared__ __attribute__((aligned(16))) char smem[];
  const int tid = threadIdx.x, lane = tid & 63;
  const int w = __builtin_amdgcn_readfirstlane(tid >> 6);
  const int g = (blockIdx.x >> 3) & 7, tg = (blockIdx.x & 7) * 4 + (blockIdx.x >> 6);
  const unsigned lds0 = (unsigned)(uintptr_t)smem;
  const int c = lane & 15, q = lane >> 4;
  STAMP_DECL;
  STAMP_R(0);
  if (w >= 4) {
    asm volatile("s_barrier" ::: "memory");
    if (g == 7) {
      for (int k = tid - 256; k < 576; k += 256) {
        const u32x4* a = (const u32x4*)(wq + (size_t)k * D_ + 560);
        const u32x4 a0 = a[0], a1 = a[1];
        const unsigned* kp = (const unsigned*)(wk + k * 18);
        const unsigned* vp = (const unsigned*)(wv + k * 18);
        const u32x4 k0 = *(const u32x4_a4*)kp, k1 = *(const u32x4_a4*)(kp + 4);
        const unsigned k2 = kp[8];
        const u32x4 v0 = *(const u32x4_a4*)vp, v1 = *(const u32x4_a4*)(vp + 4);
        const unsigned v2 = vp[8];
        char* d = smem + ((k >> 2) ^ ((k >> 3) & 1)) * 800 + (k & 3) * 192;
        *(u32x4*)(d) = a0; *(u32x4*)(d + 16) = a1;
        *(u32x4*)(d + 32) = k0; *(u32x4*)(d + 48) = k1; *(unsigned*)(d + 64) = k2;
        *(u32x4*)(d + 96) = v0; *(u32x4*)(d + 112) = v1; *(unsigned*)(d + 128) = v2;
      }
      BARRIER_LGKM();
      return;
    }
    if (lane < 48) {
      const unsigned row = lane / 12u, voff = row * 1152u + (lane - row * 12u) * 16u;
      const char* base = (const char*)wq + ((160 * g) & ~63) + ((w & 3) ^ ((w >> 1) & 1)) * 4608;
      int u = K1ROT * (tg & 3) * 9;
#pragma unroll 4
      for (int i = 0; i < 36; ++i) {
        glds16(base + u * 18432, voff, lds0 + (w & 3) * 800 + u * 3200);
        u = u == 35 ? 0 : u + 1;
      }
    }
    STAMP_R(4);
    const int job = (tg * 7 + g) * 3 + w - 5;
    if (w == 4 || job >= 648) {
      asm volatile("s_waitcnt vmcnt(0)\n\ts_barrier" ::: "memory");
      return;
    }
    const int j = job >> 2;
    const unsigned ra = j < 144 ? 16 : j < 160 ? 18 : 144, rb = j < 160 ? 2 : 18, rc = j < 144 ? 32 * j : j < 160 ? 36 * (j - 144) : 288 * (j - 160) + 16;
    const char* base = (const char*)(j < 144 ? w2 : wo) + (size_t)rc * 1152 + (job & 3) * 288;
#pragma unroll
    for (int v = 0; v < 9; ++v) {
      const unsigned ch = v * 64 + lane, row = ch / 18u, cc = ch - row * 18u;
      const unsigned src = ra * (row >> 4) + rb * ((row & 15) >> 1) + (row & 1);
      glds16(base, src * 1152u + cc * 16u, lds0 + K1_PK + (w - 5) * 9216 + v * 1024);
    }
    STAMP_R(5);
    asm volatile("s_waitcnt vmcnt(9)\n\ts_barrier\n\ts_waitcnt vmcnt(0)" ::: "memory");
    STAMP_R(2);
    const lds_cptr pk = (lds_cptr)smem + K1_PK + (w - 5) * 9216 + (16 * (q >> 1) + 4 * (q & 1) + (c >> 2)) * 288;
    char* wp = ws + WS_WP + (size_t)((job & 3) * 162 + j) * 9216 + lane * 16;
#pragma unroll
    for (int b = 0; b < 9; ++b) {
      const lds_cptr a = pk + (b < 8 ? 64 * (b >> 1) + 8 * (b & 1) + 16 * (c & 3) : 256 + 16 * (c & 1) + 8 * ((c >> 1) & 1));
      store16_wt(wp + b * 1024, __builtin_bit_cast(u32x4, cat(trread(a), trread(a + 8 * 288))));
    }
    STAMP_R(3);
    STAMP_FLUSH(0, 0, 7);
    return;
  }
  const int tok = tg * 64 + w * 16 + c;
  u32x4 xr[18];
  {
    const char* xp = (const char*)x + (tg * 64 + w * 16 + (c & 12)) * 1152 + q * 64 + (c & 3) * 16;
#pragma unroll
    for (int i = 0; i < 16; ++i) xr[i] = *(const u32x4*)(xp + (i & 3) * 1152 + (i >> 2) * 256);
#pragma unroll
    for (int i = 16; i < 18; ++i) xr[i] = *(const u32x4*)((const char*)x + tok * 1152 + i * 64 + q * 16);
    asm volatile("s_barrier" ::: "memory");
    STAMP_R(6);
    typedef __attribute__((address_space(3))) u32x4* lds_v4;
    const lds_ptr tw = (lds_ptr)smem + K1_XT + w * 4352 + (c & 12) * 272 + q * 64 + (c & 3) * 16, tr = (lds_ptr)smem + K1_XT + w * 4352 + c * 272 + q * 16;
#pragma unroll
    for (int gq = 0; gq < 4; ++gq) {
#pragma unroll
      for (int i = 0; i < 4; ++i) *(lds_v4)(tw + i * 272) = xr[4 * gq + i];
      asm volatile("" ::: "memory");
#pragma unroll
      for (int i = 0; i < 4; ++i) xr[4 * gq + i] = *(lds_v4)(tr + i * 64);
      asm volatile("" ::: "memory");
    }
    STAMP_R(4);
  }
  f32x2 d[72];
  f32x2 s2[2] = {{0.f, 0.f}, {0.f, 0.f}};
#pragma unroll
  for (int i = 0; i < 72; ++i) { d[i] = f32x2{bflo(xr[i >> 2][i & 3]), bfhi(xr[i >> 2][i & 3])}; s2[i & 1] += d[i]; }
  const float mean = quad_sum((s2[0][0] + s2[0][1]) + (s2[1][0] + s2[1][1])) * (1.f / 576.f);
  STAMP_R(5);
  f32x2 v2[2] = {{0.f, 0.f}, {0.f, 0.f}};
#pragma unroll
  for (int i = 0; i < 72; ++i) { d[i] -= mean; v2[i & 1] += d[i] * d[i]; }
  const float rstd = 1.f / sqrtf(quad_sum((v2[0][0] + v2[0][1]) + (v2[1][0] + v2[1][1])) * (1.f / 576.f) + 1e-5f);
#pragma unroll
  for (int i = 0; i < 72; ++i) { const f32x2 y = d[i] * rstd; xr[i >> 2][i & 3] = cvtpk(y[0], y[1]); }
  {
    char* xn = ws + WS_XN + (size_t)(tok >> 5) * 36864 + (w & 1) * 1024 + lane * 16;
#pragma unroll
    for (int i = 0; i < 18; ++i)
      if ((i & 7) == g) store16_wt(xn + i * 2048, xr[i]);
  }
  STAMP_R(1);
  asm volatile("s_barrier" ::: "memory");
  STAMP_R(2);
  f32x4 acc[5];
#pragma unroll
  for (int b = 0; b < 5; ++b) acc[b] = f32x4{0.f, 0.f, 0.f, 0.f};
  const int co = (c >> 2) * 192 + 8 * (c & 3) + (g < 7 ? (g & 1) * 32 : 0);
  const lds_cptr p0 = (lds_cptr)smem + (2 * q + (q & 1)) * 800 + co, p1 = (lds_cptr)smem + (2 * q + 1 - (q & 1)) * 800 + co;
  const lds_cptr p2 = p0 + 57600, p3 = p1 + 57600;
  bf16x4 al[10], ah[10];
#define K1_RD(n) do { al[(n) % 10] = trread(((n) < 45 ? p0 : p2) + ((n) % 45 / 5) * 6400 + ((n) % 5) * 32); \
                      ah[(n) % 10] = trread(((n) < 45 ? p1 : p3) + ((n) % 45 / 5) * 6400 + ((n) % 5) * 32); } while (0)
#pragma unroll
  for (int n = 0; n < 9; ++n) K1_RD(n);
  if (g < 7) {
#pragma unroll
    for (int n = 0; n < 90; ++n) {
      if (n + 9 < 90) K1_RD(n + 9);
      acc[n % 5] = MFMA16(cat(al[n % 10], ah[n % 10]), __builtin_bit_cast(bf16x8, xr[n / 5]), acc[n % 5]);
      SBAR();
    }
  } else {
#pragma unroll
    for (int n = 0; n < 90; ++n) {
      if (n + 9 < 90) K1_RD(n + 9);
      if (n % 5 < 3) acc[n % 5] = MFMA16(cat(al[n % 10], ah[n % 10]), __builtin_bit_cast(bf16x8, xr[n / 5]), acc[n % 5]);
      else acc[n % 5] = MFMA16(__builtin_bit_cast(bf16x8, xr[n / 5]), cat(al[n % 10], ah[n % 10]), acc[n % 5]);
      SBAR();
    }
  }
#undef K1_RD
  STAMP_R(7);
  constexpr float QSCALE = 0.23570226039551584f * 1.4426950408889634f;
  char* qp = ws + WS_Q + ((size_t)tok * D_ + 80 * g + 4 * q) * 2;
#define QST(b) *(u32x2*)(qp + (b) * 32) = u32x2{cvtpk(acc[b][0] * QSCALE, acc[b][1] * QSCALE), cvtpk(acc[b][2] * QSCALE, acc[b][3] * QSCALE)}
  QST(0);
  if (g < 7) {
    QST(1); QST(2); QST(3); QST(4);
  } else {
    char* kv = ws + WS_KV + (tok >> 8) * KV_HSG;
    const int kk = tok & 255;
    const u32x2 kA = {cvtpk(acc[1][0], acc[1][1]), cvtpk(acc[1][2], acc[1][3])};
    const unsigned kB = cvtpk(acc[2][0], acc[2][1]);
    *(u32x2*)(kv + (kk >> 5) * 1024 + ((q >> 1) * 32 + (kk & 31)) * 16 + (q & 1) * 8) = kA;
    if (q == 0) *(u32x2*)(kv + KV_K2 + kk * 8) = u32x2{kB, 0x3F803F80u};
    const int t0 = (tg * 64 + w * 16) & 255;
    char* vp = kv + KV_V + (t0 >> 4) * 624 + (q & 1) * 304 + (q >> 1) * 8;
    *(u32x2*)(vp + c * 16) = u32x2{cvtpk(acc[3][0], acc[3][1]), cvtpk(acc[3][2], acc[3][3])};
    if (c < 2) *(u32x2*)(vp + (16 + c) * 16) = u32x2{cvtpk(acc[4][0], acc[4][1]), cvtpk(acc[4][2], acc[4][3])};
    if (c == 2) *(u32x2*)(vp + 18 * 16) = u32x2{0x3F803F80u, 0x3F803F80u};
    if (c == 3 && (q & 1)) *(u32x2*)(vp + 19 * 16) = u32x2{0u, 0u};
    float n2 = bflo(kA[0]) * bflo(kA[0]) + bfhi(kA[0]) * bfhi(kA[0]) + bflo(kA[1]) * bflo(kA[1]) + bfhi(kA[1]) * bfhi(kA[1]);
    if (q == 0) n2 += bflo(kB) * bflo(kB) + bfhi(kB) * bfhi(kB);
    n2 = quad_sum(n2);
    n2 = fmaxf(n2, __shfl_xor(n2, 1)); n2 = fmaxf(n2, __shfl_xor(n2, 2));
    n2 = fmaxf(n2, __shfl_xor(n2, 4)); n2 = fmaxf(n2, __shfl_xor(n2, 8));
    if (lane == 0) ((float*)(ws + WS_KMAX))[tg * 4 + w] = n2;
  }
#undef QST
  STAMP_R(3);
  STAMP_FLUSH(0, 0, 7);
}

#define HSTORE(p, v) store16_wt(p, v)
constexpr int K2_FLAG = 4 * KV_HS;
constexpr int K2_LDS = K2_FLAG + 16;

#define KV_LOADS "s_mov_b32 m0, %0\n\ts_nop 0\n\t" \
               "global_load_lds_dwordx4 %1, %3\n\tglobal_load_lds_dwordx4 %1, %3 offset:1024\n\t" \
               "global_load_lds_dwordx4 %1, %3 offset:2048\n\tglobal_load_lds_dwordx4 %1, %3 offset:3072\n\t" \
               "s_add_u32 m0, %0, 0x1000\n\ts_mov_b32 exec_hi, %4\n\tglobal_load_lds_dwordx4 %2, %3\n\ts_mov_b32 exec_hi, -1\n"
DI void kv_issue(const char* src, unsigned va, unsigned vb, unsigned dst, int m5hi) {
  asm volatile(KV_LOADS ::"s"(dst), "v"(va), "v"(vb), "s"(src), "s"(m5hi) : "memory", "scc");
}
#define PFHS "6"
DI void kv_turn(int hs, const char* src, unsigned va, unsigned vb, unsigned dst, int m5hi, char* box, unsigned voff, u32x4 ones) {
  asm volatile("s_cmp_lt_u32 %5, 5\n\ts_cbranch_scc0 1f\n\ts_waitcnt vmcnt(5)\n\ts_barrier\n\t" KV_LOADS "\ts_branch 2f\n"
               "1:\n\ts_waitcnt vmcnt(0)\n\ts_barrier\n\ts_cmp_lg_u32 %5, " PFHS "\n\ts_cbranch_scc1 2f\n\t"
               "global_store_dwordx4 %7, %8, %6 offset:-3072 sc1\n\tglobal_store_dwordx4 %7, %8, %6 offset:-2048 sc1\n\t"
               "global_store_dwordx4 %7, %8, %6 offset:-1024 sc1\n\tglobal_store_dwordx4 %7, %8, %6 sc1\n\t"
               "global_store_dwordx4 %7, %8, %6 offset:1024 sc1\n\tglobal_store_dwordx4 %7, %8, %6 offset:2048 sc1\n\t"
               "global_store_dwordx4 %7, %8, %6 offset:3072 sc1\n"
               "2:"
               ::"s"(dst), "v"(va), "v"(vb), "s"(src), "s"(m5hi), "s"(hs), "s"(box), "v"(voff), "v"(ones) : "memory", "scc");
}
#undef KV_LOADS
DI bf16x4 q_tail(unsigned q2, float m, int h) {
  const float m1 = bflo(cvtpk(m, 0.f)), r1 = m - m1, m2 = bflo(cvtpk(r1, 0.f)), m3 = r1 - m2;
  return __builtin_bit_cast(bf16x4, h ? u32x2{0u, cvtpk(-m3, 0.f)} : u32x2{q2, cvtpk(-m1, -m2)});
}

DI void kv_first(char* smem, const char* kvg, int w, int lane) {
  const unsigned va = w * 5120 + lane * 16;
#pragma unroll
  for (int i = 0; i < 3; ++i) kv_issue(kvg + i * KV_HSG, va, va + 4096, (unsigned)(uintptr_t)smem + w * 5120 + i * KV_HS, w == 3 ? 0xFFFF : -1);
}

#define APRIO_HI 2
#define APRIO_LO 0
DI void attn_main(char* ws, char* smem, const char* kvg, char* box, int w, int lane, bf16x8 qa1, bf16x4 qa2, bf16x8 qb1, bf16x4 qb2, f32x16& oa, f32x16& ob) {
  const unsigned lds0 = (unsigned)(uintptr_t)smem;
  const int r = lane & 31, h = lane >> 5;
  const unsigned va = w * 5120 + lane * 16, vb = va + 4096, dst = lds0 + w * 5120;
  const int m5 = w == 3 ? 0xFFFF : -1;
  const int ok1 = lane * 16, ok2 = KV_K2 + r * 8, ov = KV_V + (r < 19 ? h * 304 + r * 16 : 608);
  const f32x16 zero = {};
  u32x4 ones = {~0u, ~0u, ~0u, ~0u};
  PIN(ones);
  STAMP_DECL;
  STAMP_R(2);
  asm volatile("s_waitcnt vmcnt(10)\n\ts_barrier" ::: "memory");
  STAMP_R(3);
  bf16x8 k1 = LDS128((lds_cptr)smem + ok1), v0 = LDS128((lds_cptr)smem + ov), v1 = {};
  bf16x4 k2 = LDS64((lds_cptr)smem + ok2);
  f32x16 sa = MFMA32(k1, qa1, MFMA32K8(k2, qa2, zero)), sb;
  u32x4 p0, p1 = {0u, 0u, 0u, 0u};
  float e2 = 0.f, e3 = 0.f;
#define SLOT(MF, RD, NEXT, S, i, PP, jp, P, j) do { \
    MF; SBAR(); __builtin_amdgcn_s_setprio(APRIO_LO); SBAR(); \
    float e0 = __builtin_amdgcn_exp2f(S[i]); PIN(e0); SBAR(); RD; SBAR(); \
    float e1 = __builtin_amdgcn_exp2f(S[i + 1]); PIN(e1); SBAR(); \
    PP[jp] = cvtpk(e2, e3); PIN(PP); SBAR(); \
    e2 = __builtin_amdgcn_exp2f(S[i + 2]); PIN(e2); SBAR(); PIN(NEXT); SBAR(); \
    e3 = __builtin_amdgcn_exp2f(S[i + 3]); PIN(e3); SBAR(); __builtin_amdgcn_s_setprio(APRIO_HI); SBAR(); \
    P[j] = cvtpk(e0, e1); PIN(P); SBAR(); } while (0)
#define PB(x) __builtin_bit_cast(bf16x8, x)
  for (int hs = 0; hs < 8; ++hs) {
    const lds_cptr cur = (lds_cptr)smem + (hs & 3) * KV_HS, nxt = (lds_cptr)smem + ((hs + 1) & 3) * KV_HS;
    const lds_cptr ck1 = cur + ok1, ck2 = cur + ok2, cv = cur + ov, nk1 = nxt + ok1, nk2 = nxt + ok2, nv = nxt + ov;
#pragma unroll
    for (int t = 0; t < 8; ++t) {
      if (t == 7) kv_turn(hs, kvg + ((hs + 3) & 7) * KV_HSG, va, vb, dst + ((hs + 3) & 3) * KV_HS, m5, box, ok1, ones);
      SBAR();
      SLOT(sb = MFMA32K8(k2, qb2, zero), k2 = t < 7 ? LDS64(ck2 + (t + 1) * 256) : LDS64(nk2), e0, sa, 0, p1, 3, p0, 0);
      SLOT(ob = MFMA32(v1, PB(p1), ob), v1 = LDS128(cv + (2 * t + 1) * 624), e0, sa, 4, p0, 1, p0, 2);
      SLOT(sb = MFMA32(k1, qb1, sb), k1 = t < 7 ? LDS128(ck1 + (t + 1) * 1024) : LDS128(nk1), v0, sa, 8, p0, 3, p1, 0);
      SLOT(oa = MFMA32(v0, PB(p0), oa), , k2, sa, 12, p1, 1, p1, 2);
      SLOT(sa = MFMA32K8(k2, qa2, zero), , v1, sb, 0, p1, 3, p0, 0);
      SLOT(oa = MFMA32(v1, PB(p1), oa), , k1, sb, 4, p0, 1, p0, 2);
      SLOT(sa = MFMA32(k1, qa1, sa), , e0, sb, 8, p0, 3, p1, 0);
      SLOT(ob = MFMA32(v0, PB(p0), ob), v0 = t < 7 ? LDS128(cv + (2 * t + 2) * 624) : LDS128(nv), e0, sb, 12, p1, 1, p1, 2);
    }
  }
#undef SLOT
  STAMP_R(4);
  STAMP_FLUSH(1, 2, 4);
  p1[3] = cvtpk(e2, e3);
  ob = MFMA32(v1, PB(p1), ob);
#undef PB
  asm volatile("s_waitcnt vmcnt(0) lgkmcnt(0)\n\ts_barrier" ::: "memory");
}

DI void attn_max(char* smem, const char* kvg, int w, int lane, bf16x8 qa1, bf16x4 qa2, bf16x8 qb1, bf16x4 qb2, float& ma, float& mb) {
  const unsigned lds0 = (unsigned)(uintptr_t)smem;
  const unsigned va = w * 5120 + lane * 16;
  const f32x16 zero = {};
  for (int hs = 0; hs < 8; ++hs) {
    kv_issue(kvg + hs * KV_HSG, va, va + 4096, lds0 + w * 5120, w == 3 ? 0xFFFF : -1);
    asm volatile("s_waitcnt vmcnt(0)\n\ts_barrier" ::: "memory");
#pragma unroll 1
    for (int t = 0; t < 8; ++t) {
      const bf16x8 k1 = LDS128((lds_cptr)smem + lane * 16 + t * 1024);
      const bf16x4 k2 = LDS64((lds_cptr)smem + KV_K2 + (lane & 31) * 8 + t * 256);
      const f32x16 sa = MFMA32(k1, qa1, MFMA32K8(k2, qa2, zero)), sb = MFMA32(k1, qb1, MFMA32K8(k2, qb2, zero));
#pragma unroll
      for (int i = 0; i < 16; ++i) { ma = fmaxf(ma, sa[i]); mb = fmaxf(mb, sb[i]); }
    }
    BARRIER_LGKM();
  }
  ma = fmaxf(ma, __shfl_xor(ma, 32));
  mb = fmaxf(mb, __shfl_xor(mb, 32));
}

DI void attn_store(char* hp, int hd, int lane, const f32x16& o, float l) {
  const float inv = 1.f / l;
  HSTORE(hp + (288 + hd) * 1024 + lane * 16,
         (u32x4{cvtpk(o[0] * inv, o[1] * inv), cvtpk(o[2] * inv, o[3] * inv), cvtpk(o[4] * inv, o[5] * inv), cvtpk(o[6] * inv, o[7] * inv)}));
  if (lane < 32) {
    const int p = 2 * (hd & 7);
    *(unsigned*)(hp + (320 + (hd >> 3)) * 1024 + (((p >> 2) & 1) * 32 + lane) * 16 + 2 * (4 * (p >> 3) + (p & 3))) = cvtpk(o[8] * inv, o[9] * inv);
  }
}

DI float half_sum(float v) {
  const auto t = __builtin_amdgcn_permlane32_swap(__float_as_uint(v), __float_as_uint(v), false, false);
  return __uint_as_float(t[0]) + __uint_as_float(t[1]);
}
DI void attn_unit(char* smem, char* ws, int u, int w, int lane) {
  const char* kvg = ws + WS_KV;
  kv_first(smem, kvg, w, lane);
  const int gw = u * 4 + w, hd = (gw & 15) * 2, tb = gw >> 4;
  const int r = lane & 31, h = lane >> 5;
  const char* qrow = ws + WS_Q + ((size_t)(tb * 32 + r) * D_ + 18 * hd) * 2;
  const u32x4 qa = *(const u32x4_a4*)(qrow + 16 * h), qb = *(const u32x4_a4*)(qrow + 36 + 16 * h);
  const unsigned qa2 = *(const unsigned*)(qrow + 32), qb2 = *(const unsigned*)(qrow + 68);
  const u32x2 kx = *(const u32x2*)(ws + WS_KMAX + lane * 8);
  unsigned ku = max(kx[0], kx[1]);
  ku = max(ku, (unsigned)__builtin_amdgcn_update_dpp(0, (int)ku, 0xB1, 0xF, 0xF, true));
  ku = max(ku, (unsigned)__builtin_amdgcn_update_dpp(0, (int)ku, 0x4E, 0xF, 0xF, true));
  ku = max(ku, (unsigned)__builtin_amdgcn_update_dpp(0, (int)ku, 0x141, 0xF, 0xF, true));
  ku = max(ku, (unsigned)__builtin_amdgcn_update_dpp(0, (int)ku, 0x140, 0xF, 0xF, true));
  const float km = __uint_as_float(max(max((unsigned)__builtin_amdgcn_readlane(ku, 0), (unsigned)__builtin_amdgcn_readlane(ku, 16)),
                                       max((unsigned)__builtin_amdgcn_readlane(ku, 32), (unsigned)__builtin_amdgcn_readlane(ku, 48))));
  float na = 0.f, nb = 0.f;
#pragma unroll
  for (int j = 0; j < 4; ++j) {
    na += bflo(qa[j]) * bflo(qa[j]) + bfhi(qa[j]) * bfhi(qa[j]);
    nb += bflo(qb[j]) * bflo(qb[j]) + bfhi(qb[j]) * bfhi(qb[j]);
  }
  na = half_sum(na) + bflo(qa2) * bflo(qa2) + bfhi(qa2) * bfhi(qa2);
  nb = half_sum(nb) + bflo(qb2) * bflo(qb2) + bfhi(qb2) * bfhi(qb2);
  float ma = sqrtf(na * km) * 1.001f, mb = sqrtf(nb * km) * 1.001f;
  volatile __attribute__((address_space(3))) int* flag = (volatile __attribute__((address_space(3))) int*)((lds_ptr)smem + K2_FLAG);
  if (w == 0 && lane == 0) *flag = 0;
  const bf16x8 qa1 = __builtin_bit_cast(bf16x8, qa), qb1 = __builtin_bit_cast(bf16x8, qb);
  f32x16 oa, ob;
  float la, lb;
  for (int attempt = 0;; ++attempt) {
#pragma unroll
    for (int i = 0; i < 16; ++i) { oa[i] = 0.f; ob[i] = 0.f; }
    attn_main(ws, smem, kvg, ws + WS_P + (size_t)gw * 7168 + 3072, w, lane, qa1, q_tail(qa2, ma, h), qb1, q_tail(qb2, mb, h), oa, ob);
    la = __shfl(oa[10], r); lb = __shfl(ob[10], r);
    if (attempt) break;
    if (!(la >= 8.2718061e-25f && lb >= 8.2718061e-25f)) *flag = 1;
    BARRIER_LGKM();
    const int bad = *flag;
    if (!__builtin_amdgcn_readfirstlane(bad)) break;
    ma = -INFINITY; mb = -INFINITY;
    attn_max(smem, kvg, w, lane, qa1, q_tail(qa2, 0.f, h), qb1, q_tail(qb2, 0.f, h), ma, mb);
    kv_first(smem, kvg, w, lane);
  }
  char* hp = ws + WS_H + (size_t)tb * NBLK * 1024;
  attn_store(hp, hd, lane, oa, la);
  attn_store(hp, hd + 1, lane, ob, lb);
}

DI float silu_mul(float g, float u) {
  const float e = __builtin_amdgcn_exp2f(g * -1.4426950408889634f);
  return g * __builtin_amdgcn_rcpf(1.f + e) * u;
}

constexpr int G1_W3 = 9216, G1_SLOT = G1_W3 + 9216;
DI void glds16h(const void* sbase, unsigned voff, unsigned lds_addr) {
  asm volatile("s_mov_b32 m0, %0\n\ts_mov_b32 exec_hi, 0\n\tglobal_load_lds_dwordx4 %1, %2\n\ts_mov_b32 exec_hi, -1" ::"s"(lds_addr), "v"(voff), "s"(sbase) : "memory");
}
template <int OFF>
DI void gload16(u32x4& a, const void* sbase, unsigned voff) {
  asm volatile("global_load_dwordx4 %0, %1, %2 offset:%3" : "+v"(a) : "v"(voff), "s"(sbase), "i"(OFF) : "memory");
}

DI void gemm1_unit(char* smem, char* ws, const unsigned short* w1, const unsigned short* w3, const unsigned short* w2, int u, int w, int lane) {
  const unsigned lds0 = (unsigned)(uintptr_t)smem;
  const int j = u >> 3, ct = (u & 7) * 4 + (j & 3), rt = j >> 2;
  const char* wsrc = (const char*)((w >> 1) ? w3 : w1) + (w & 1) * 16 * 9216 + ct * 288;
  const unsigned wdst = lds0 + (w >> 1) * G1_W3 + (w & 1) * 4608;
  unsigned woff[5];
#pragma unroll
  for (int i = 0; i < 5; ++i) { const unsigned ch = i * 64 + (i < 4 ? lane : lane & 31), row = ch / 18u; woff[i] = (row ^ ((row >> 1) & 4u)) * 9216u + (ch - row * 18u) * 16u; }
  const char* xsrc = ws + WS_XN + (size_t)(rt * 4 + w) * 36864;
  const unsigned xoff = lane * 16;
  int sw = rt * 9 / 4, sx = sw;
  u32x4 xf[3][2];
#pragma unroll
  for (int i = 0; i < 3; ++i) { xf[i][0] = u32x4{0, 0, 0, 0}; xf[i][1] = u32x4{0, 0, 0, 0}; }
#define G1_WP(slot, i) do { if ((i) < 4) glds16(wsrc + (size_t)sw * 294912, woff[i], wdst + (slot) * G1_SLOT + (i) * 1024); \
    else { glds16h(wsrc + (size_t)sw * 294912, woff[4], wdst + (slot) * G1_SLOT + 4096); sw = sw == 17 ? 0 : sw + 1; } } while (0)
#define G1_XP(set, i) do { if ((i) == 0) gload16<0>(xf[set][0], xsrc + sx * 2048, xoff); \
    else { gload16<1024>(xf[set][1], xsrc + sx * 2048, xoff); sx = sx == 17 ? 0 : sx + 1; } } while (0)
#pragma unroll
  for (int s = 0; s < 3; ++s) {
#pragma unroll
    for (int i = 0; i < 5; ++i) G1_WP(s, i);
    if (s) { G1_XP(s - 1, 0); G1_XP(s - 1, 1); }
  }
  const int c = lane & 15, q = lane >> 4;
  const int aoff0 = (8 * q + 4 * (q & 1) + (c >> 2)) * 288 + 8 * (c & 3), aoff1 = (8 * q + 4 * ((q & 1) ^ 1) + (c >> 2)) * 288 + 8 * (c & 3);
#define G1_RD(p, i) trread((p) + ((i) / 9) * G1_W3 + ((i) % 9) * 32)
  bf16x4 al[6], ah[6];
  STAMP_DECL;
  STAMP_R(2);
  asm volatile("s_waitcnt vmcnt(14)\n\ts_barrier" ::: "memory");
  STAMP_R(3);
#pragma unroll
  for (int i = 0; i < 6; ++i) { al[i] = G1_RD((lds_cptr)smem + aoff0, i); if (i < 5) ah[i] = G1_RD((lds_cptr)smem + aoff1, i); }
  int g = 0;
  f32x4 acc[36];
#define G1_STAGE(s, VM, LW, LX) do { \
    asm volatile("s_waitcnt vmcnt(" #VM ")\n\ts_barrier" : "+v"(xf[s][0]), "+v"(xf[s][1]) :: "memory"); \
    const lds_cptr pc = (lds_cptr)smem + (g & 3) * G1_SLOT, pn = (lds_cptr)smem + ((g + 1) & 3) * G1_SLOT; \
    const lds_cptr pc0 = pc + aoff0, pc1 = pc + aoff1, pn0 = pn + aoff0, pn1 = pn + aoff1; \
    SBAR(); \
    _Pragma("unroll") for (int i = 0; i < 18; ++i) { \
      MFMA16A(acc[2 * i], cat(al[i % 6], ah[i % 6]), xf[s][0]); \
      SBAR(); \
      ah[(i + 5) % 6] = i < 13 ? G1_RD(pc1, i + 5) : G1_RD(pn1, i - 13); \
      if (LW && (i & 1) && i < 10) G1_WP((g + 3) & 3, i >> 1); \
      if (LX && i == 11) { if (g == 16) xsrc += 32 * 36864; G1_XP((s + 2) % 3, 0); } \
      if (LX && i == 13) G1_XP((s + 2) % 3, 1); \
      SBAR(); \
      MFMA16A(acc[2 * i + 1], cat(al[i % 6], ah[i % 6]), xf[s][1]); \
      SBAR(); \
      al[i % 6] = i < 12 ? G1_RD(pc0, i + 6) : G1_RD(pn0, i - 12); \
      SBAR(); \
    } \
    ++g; } while (0)
  for (int un = 0; un < 2; ++un) {
#pragma unroll
    for (int b = 0; b < 36; ++b) acc[b] = f32x4{0.f, 0.f, 0.f, 0.f};
    for (int trip = un; trip < 6; ++trip) { G1_STAGE(0, 7, true, true); G1_STAGE(1, 7, true, true); G1_STAGE(2, 7, true, true); }
    if (un) { G1_STAGE(0, 7, false, true); G1_STAGE(1, 2, false, false); G1_STAGE(2, 0, false, false); }
    MFMA_DRAIN();
    if (un == 0) STAMP_R(4); else STAMP_R(6);
    char* hp = ws + WS_H + ((size_t)(rt * 4 + un * 32 + w) * NBLK + ct * 9) * 1024 + ((q & 1) * 32 + (q >> 1) * 16 + c) * 16;
#pragma unroll
    for (int b = 0; b < 9; ++b) {
      PIN(acc[2 * b]); PIN(acc[2 * b + 1]); PIN(acc[18 + 2 * b]); PIN(acc[19 + 2 * b]);
      unsigned t[2][2];
#pragma unroll
      for (int i = 0; i < 4; ++i) {
        const f32x4 &ga = acc[2 * b + (i >> 1)], &ua = acc[18 + 2 * b + (i >> 1)];
        t[i >> 1][i & 1] = cvtpk(silu_mul(ga[2 * (i & 1)], ua[2 * (i & 1)]), silu_mul(ga[2 * (i & 1) + 1], ua[2 * (i & 1) + 1]));
      }
      const auto s0 = __builtin_amdgcn_permlane32_swap(t[0][0], t[1][0], false, false), s1 = __builtin_amdgcn_permlane32_swap(t[0][1], t[1][1], false, false);
      HSTORE(hp + b * 1024, (u32x4{s0[0], s1[0], s0[1], s1[1]}));
    }
    if (un == 0) STAMP_R(5);
  }
  STAMP_FLUSH(K2MODE ? 4 : 1, 2, 6);
#undef G1_WP
#undef G1_XP
#undef G1_RD
#undef G1_STAGE
}

template <int MODE>
__global__ __launch_bounds__(256, 2) void k2_attn_gemm1(const unsigned short* __restrict__ w1, const unsigned short* __restrict__ w3,
                                                        const unsigned short* __restrict__ w2, char* __restrict__ ws) {
  extern __shared__ __attribute__((aligned(16))) char smem[];
  const int lane = threadIdx.x & 63;
  const int w = __builtin_amdgcn_readfirstlane(threadIdx.x >> 6);
  const int u = blockIdx.x;
  STAMP_AT(1 + (MODE == 2) * 3, 0);
  if (MODE == 1) { attn_unit(smem, ws, u, w, lane); STAMP_AT(1, 1); return; }
  if (MODE == 2) { gemm1_unit(smem, ws, w1, w3, w2, u, w, lane); STAMP_AT(4, 1); return; }
  if (u < 256) {
#define GPRIO 1
    __builtin_amdgcn_s_setprio(GPRIO);
    gemm1_unit(smem, ws, w1, w3, w2, u, w, lane);
  } else {
    attn_unit(smem, ws, u - 256, w, lane);
  }
  STAMP_AT(1, 1);
}

#define PSTORE(p, v) store16_wt(p, v)
#define PSLEEP 1
constexpr int G2_STAGE = 18432, K3_LDS = 8 * G2_STAGE;
template <int OFF>
DI void bload16(u32x4& a, __amdgpu_buffer_rsrc_t r, unsigned voff, unsigned soff) {
  asm volatile("buffer_load_dwordx4 %0, %1, %2, %3 offen offset:%4" : "=v"(a) : "v"(voff), "s"(r), "s"(soff), "i"(OFF) : "memory");
}
template <int OFF>
DI void blds16(__amdgpu_buffer_rsrc_t r, unsigned voff, unsigned soff) {
  asm volatile("buffer_load_dwordx4 %0, %1, %2 offen offset:%3 lds" ::"v"(voff), "s"(r), "s"(soff), "i"(OFF) : "memory");
}
template <int OFF>
DI void blds4(__amdgpu_buffer_rsrc_t r, unsigned voff, unsigned soff) {
  asm volatile("buffer_load_dword %0, %1, %2 offen offset:%3 lds" ::"v"(voff), "s"(r), "s"(soff), "i"(OFF) : "memory");
}
template <int ADD>
DI void set_m0(unsigned base) { asm volatile("s_add_u32 m0, %0, %1" ::"s"(base), "i"(ADD) : "memory", "scc"); }
template <int N>
DI void wait_bar(u32x4& a, u32x4& b) { asm volatile("s_waitcnt vmcnt(%2)\n\ts_barrier" : "+v"(a), "+v"(b) : "i"(N) : "memory"); }

template <int T>
DI void g2_iter(f32x4 (&acc)[2][9], bf16x8 (&af)[9], u32x4 (&hf)[6][2], const lds_cptr (&ap)[3], __amdgpu_buffer_rsrc_t rw, __amdgpu_buffer_rsrc_t rh,
                unsigned w16, unsigned w4, unsigned hoff, unsigned hoff1, unsigned wdst) {
  constexpr int X = T + 1, NH = (T + 4 < 20 ? T + 4 : 20) - X + 1, NW = (T + 5 < 20 ? T + 5 : 20) - X;
  wait_bar<(T == 20 ? 0 : 2 * NH + 3 * NW)>(hf[T % 6][0], hf[T % 6][1]);
  SBAR();
#pragma unroll
  for (int b = 0; b < 9; ++b) {
    MFMA16A(acc[0][b], af[b], hf[T % 6][0]);
    SBAR();
    if (T + 5 <= 20 && b == 0) bload16<0>(hf[(T + 5) % 6][0], rh, hoff, (T + 5) * 4096);
    if (T + 5 <= 20 && b == 1) bload16<0>(hf[(T + 5) % 6][1], rh, hoff1, (T + 5) * 4096);
    if (T + 6 <= 20 && b == 2) set_m0<((T + 6) & 7) * G2_STAGE>(wdst);
    if (T + 6 <= 20 && b == 3) blds16<0>(rw, w16, (T + 6) * G2_STAGE);
    if (T + 6 <= 20 && b == 5) blds16<1024>(rw, w16, (T + 6) * G2_STAGE);
    if (T + 6 <= 20 && b == 7) blds4<2048>(rw, w4, (T + 6) * G2_STAGE);
    SBAR();
    MFMA16A(acc[1][b], af[b], hf[T % 6][1]);
    SBAR();
    if (T < 20) af[b] = LDS128(ap[((T + 1) & 7) / 3] + (((T + 1) & 7) % 3) * G2_STAGE + b * 1024);
    SBAR();
  }
}
template <int T, int E, typename... A>
DI void g2_run(A&... a) { if constexpr (T < E) { g2_iter<T>(a...); g2_run<T + 1, E>(a...); } }

__global__ __launch_bounds__(512, 2) void k3_gemm2(const unsigned short* __restrict__ x, char* __restrict__ ws, unsigned short* __restrict__ out) {
  extern __shared__ __attribute__((aligned(16))) char smem[];
  const unsigned lds0 = (unsigned)(uintptr_t)smem;
  const int lane = threadIdx.x & 63;
  const int w = __builtin_amdgcn_readfirstlane(threadIdx.x >> 6);
  const int xcd = blockIdx.x & 7, j = blockIdx.x >> 3;
  const int sp = xcd >> 1, rt = (xcd & 1) * 8 + (j >> 2), ct = j & 3;
  const int wm = w & 3, kg = w >> 2;
  const int c = lane & 15, q = lane >> 4;
  STAMP_DECL;
  STAMP_R(0);
  const __amdgpu_buffer_rsrc_t rw = __builtin_amdgcn_make_buffer_rsrc(ws + WS_WP + (size_t)(ct * 81 + sp * 20) * G2_STAGE + w * 2304, 0, 21 * G2_STAGE, 0x00020000);
  const __amdgpu_buffer_rsrc_t rh = __builtin_amdgcn_make_buffer_rsrc(ws + WS_H + (size_t)(rt * 4 + wm) * NBLK * 1024 + sp * 81920 + kg * 2048, 0, 21 * 4096, 0x00020000);
  unsigned w16 = lane * 16, w4 = lane * 4, wdst = lds0 + w * 2304;
  unsigned hoff = (q >> 1) * 1024 + (q & 1) * 512 + c * 16 + kg * 256, hoff1 = hoff ^ 256;
  u32x4 hf[6][2];
#define G2_PW(s) do { set_m0<(s) * G2_STAGE>(wdst); asm volatile("s_nop 0"); blds16<0>(rw, w16, (s) * G2_STAGE); blds16<1024>(rw, w16, (s) * G2_STAGE); blds4<2048>(rw, w4, (s) * G2_STAGE); } while (0)
#define G2_PH(s) do { bload16<0>(hf[s][0], rh, hoff, (s) * 4096); bload16<0>(hf[s][1], rh, hoff1, (s) * 4096); } while (0)
#define G2_PB() asm volatile("s_barrier" ::: "memory")
  G2_PW(0); G2_PH(0); G2_PB(); G2_PW(1); G2_PH(1); G2_PB(); G2_PW(2); G2_PH(2); G2_PB(); G2_PW(3); G2_PH(3); G2_PB(); G2_PW(4); G2_PH(4); G2_PB(); G2_PW(5);
#undef G2_PB
#undef G2_PW
#undef G2_PH
  f32x4 acc[2][9];
#pragma unroll
  for (int i = 0; i < 2; ++i)
#pragma unroll
    for (int b = 0; b < 9; ++b) { acc[i][b] = f32x4{0.f, 0.f, 0.f, 0.f}; PIN(acc[i][b]); }
  lds_cptr ap[3];
#pragma unroll
  for (int i = 0; i < 3; ++i) { ap[i] = (lds_cptr)smem + i * 3 * G2_STAGE + kg * 9216 + lane * 16; PIN(ap[i]); }
  bf16x8 af[9];
  asm volatile("s_waitcnt vmcnt(25)\n\ts_barrier" ::: "memory");
  STAMP_R(4);
#pragma unroll
  for (int b = 0; b < 9; ++b) af[b] = LDS128(ap[0] + b * 1024);
  g2_run<0, 6>(acc, af, hf, ap, rw, rh, w16, w4, hoff, hoff1, wdst);
  g2_run<6, 14>(acc, af, hf, ap, rw, rh, w16, w4, hoff, hoff1, wdst);
  g2_run<14, 20>(acc, af, hf, ap, rw, rh, w16, w4, hoff, hoff1, wdst);
  if (sp == 3) g2_iter<20>(acc, af, hf, ap, rw, rh, w16, w4, hoff, hoff1, wdst);
  STAMP_R(1);
  asm volatile("s_nop 7\n\ts_waitcnt vmcnt(0) lgkmcnt(0)\n\ts_barrier" : "+v"(hf[20 % 6][0]), "+v"(hf[20 % 6][1]) :: "memory");
#pragma unroll
  for (int b = 0; b < 9; ++b) { PIN(acc[0][b]); PIN(acc[1][b]); }
  f32x4* red = (f32x4*)(smem + (wm * 2) * 9216) + lane;
#pragma unroll
  for (int b = 0; b < 9; ++b) red[(kg ^ 1) * 576 + b * 64] = acc[1][b];
  BARRIER_LGKM();
  STAMP_R(7);
  const size_t orow = (size_t)(rt * 128 + wm * 32 + kg * 16) * D_ + ct * 144;
  const size_t oi = orow + (lane >> 2) * D_ + sp * 32 + (lane & 3) * 8, ot = orow + ((lane & 31) >> 1) * D_ + 128 + (lane & 1) * 8;
  const u32x4 xv = *(const u32x4*)(x + oi), xt = *(const u32x4*)(x + ot);
  f32x4 v[10];
#pragma unroll
  for (int b = 0; b < 9; ++b) v[b] = acc[0][b] + red[kg * 576 + b * 64];
#pragma unroll
  for (int i = 0; i < 4; ++i) v[9][i] = __uint_as_float(__builtin_amdgcn_permlane32_swap(__float_as_uint(v[8][i]), __float_as_uint(v[8][i]), false, false)[1]);
  char* box = ws + WS_P + (size_t)(rt * 4 + ct) * (12 * 9216) + lane * 16;
#define PK8(a, b) u32x4{min(cvtpk(a[0], a[1]), ~1u), min(cvtpk(a[2], a[3]), ~1u), min(cvtpk(b[0], b[1]), ~1u), min(cvtpk(b[2], b[3]), ~1u)}
#pragma unroll
  for (int j = 0; j < 4; ++j)
    if (j != sp) store16_wt(box + (j * 3 + ((sp - j - 1) & 3)) * 9216 + w * 1024, PK8(v[2 * j], v[2 * j + 1]));
  if (wm != sp && lane < 32) store16_wt(box + (wm * 3 + ((sp - wm - 1) & 3)) * 9216 + 8192 + kg * 512, PK8(v[8], v[9]));
#undef PK8
  f32x4 lo = v[0], hi = v[1];
#pragma unroll
  for (int j = 1; j < 4; ++j)
    if (j == sp) { lo = v[2 * j]; hi = v[2 * j + 1]; }
  const char* in = box + sp * 3 * 9216 + w * 1024;
  const char* tin = box + sp * 3 * 9216 + 8192 + kg * 512;
  u32x4 r[6];
  unsigned long long m[6];
#pragma unroll
  for (int i = 0; i < 6; ++i) { r[i] = u32x4{0u, 0u, 0u, 0u}; m[i] = i < 3 ? ~0ull : wm == sp ? 0xFFFFFFFFull : 0ull; }
  STAMP_R(5);
  for (int spin = 0; spin < (1 << 20); ++spin) {
    asm volatile("s_mov_b64 exec, %12\n\tglobal_load_dwordx4 %0, %6, off sc1\n\ts_mov_b64 exec, %13\n\tglobal_load_dwordx4 %1, %7, off sc1\n\t"
                 "s_mov_b64 exec, %14\n\tglobal_load_dwordx4 %2, %8, off sc1\n\ts_mov_b64 exec, %15\n\tglobal_load_dwordx4 %3, %9, off sc1\n\t"
                 "s_mov_b64 exec, %16\n\tglobal_load_dwordx4 %4, %10, off sc1\n\ts_mov_b64 exec, %17\n\tglobal_load_dwordx4 %5, %11, off sc1\n\t"
                 "s_mov_b64 exec, -1\n\ts_waitcnt vmcnt(0)"
                 : "+v"(r[0]), "+v"(r[1]), "+v"(r[2]), "+v"(r[3]), "+v"(r[4]), "+v"(r[5])
                 : "v"(in), "v"(in + 9216), "v"(in + 18432), "v"(tin), "v"(tin + 9216), "v"(tin + 18432),
                   "s"(m[0]), "s"(m[1]), "s"(m[2]), "s"(m[3]), "s"(m[4]), "s"(m[5]) : "memory");
#pragma unroll
    for (int i = 0; i < 6; ++i) m[i] = __ballot(max(max(r[i][0], r[i][1]), max(r[i][2], r[i][3])) == ~0u);
    if (!(m[0] | m[1] | m[2] | m[3] | m[4] | m[5])) break;
    __builtin_amdgcn_s_sleep(PSLEEP);
  }
  STAMP_R(3);
#define FIN(o, xx, a, b, r0, src) _Pragma("unroll") for (int k = 0; k < 4; ++k) { \
    const float e = (k < 2 ? a[2 * k] : b[2 * k - 4]) + bflo(r[r0][k]) + (bflo(r[r0 + 1][k]) + bflo(r[r0 + 2][k])); \
    const float f = (k < 2 ? a[2 * k + 1] : b[2 * k - 3]) + bfhi(r[r0][k]) + (bfhi(r[r0 + 1][k]) + bfhi(r[r0 + 2][k])); \
    o[k] = cvtpk(bflo(xx[k]) + __shfl(e, src), bfhi(xx[k]) + __shfl(f, src)); }
  u32x4 o;
  FIN(o, xv, lo, hi, 0, (lane & 3) * 16 + (lane >> 2));
  *(u32x4*)(out + oi) = o;
  if (wm == sp) {
    FIN(o, xt, v[8], v[9], 3, (lane & 1) * 16 + ((lane & 31) >> 1));
    if (lane < 32) *(u32x4*)(out + ot) = o;
  }
#undef FIN
  STAMP_R(2);
  STAMP_FLUSH(2, 0, 7);
}

extern "C" void kernel_launch(void* const* d_in, const int* in_sizes, int n_in, void* d_out, int out_size, void* d_ws, size_t ws_size,
                              hipStream_t stream) {
  const unsigned short* x = (const unsigned short*)d_in[0];
  const unsigned short* wq = (const unsigned short*)d_in[1];
  const unsigned short* wk = (const unsigned short*)d_in[2];
  const unsigned short* wv = (const unsigned short*)d_in[3];
  const unsigned short* wo = (const unsigned short*)d_in[4];
  const unsigned short* w1 = (const unsigned short*)d_in[6];
  const unsigned short* w3 = (const unsigned short*)d_in[7];
  const unsigned short* w2 = (const unsigned short*)d_in[8];
  char* ws = (char*)d_ws;
  (void)hipFuncSetAttribute((const void*)k1_ln_qkv, hipFuncAttributeMaxDynamicSharedMemorySize, K1_LDS);
  (void)hipFuncSetAttribute((const void*)k3_gemm2, hipFuncAttributeMaxDynamicSharedMemorySize, K3_LDS);
  k1_ln_qkv<<<256, 512, K1_LDS, stream>>>(x, wq, wk, wv, wo, w2, ws);
  (void)hipFuncSetAttribute((const void*)k2_attn_gemm1<0>, hipFuncAttributeMaxDynamicSharedMemorySize, K2_LDS);
  k2_attn_gemm1<0><<<512, 256, K2_LDS, stream>>>(w1, w3, w2, ws);
  k3_gemm2<<<256, 512, K3_LDS, stream>>>(x, ws, (unsigned short*)d_out);
}
```

```cpp
#define STAMP
#define DIAG 0
#include <hip/hip_runtime.h>
#include <hip/hip_bf16.h>
#include <stdint.h>

typedef short bf16x8 __attribute__((ext_vector_type(8)));
typedef short bf16x4 __attribute__((ext_vector_type(4)));
typedef float f32x4 __attribute__((ext_vector_type(4)));
typedef float f32x16 __attribute__((ext_vector_type(16)));
typedef unsigned u32x4 __attribute__((ext_vector_type(4)));
typedef unsigned u32x2 __attribute__((ext_vector_type(2)));
typedef unsigned u32x4_a4 __attribute__((ext_vector_type(4), aligned(4)));
typedef __attribute__((address_space(3))) const char* lds_cptr;
typedef __attribute__((address_space(3))) char* lds_ptr;
#define DI __device__ __forceinline__

#define XCHG 1
#define K2MODE 0
constexpr int S_ = 2048, D_ = 576, F_ = 4608;
constexpr int NBLK = 324;
constexpr int KV_HS = 20224, KV_HSG = 20480;
constexpr int KV_K2 = 8192, KV_V = 10240;
constexpr size_t WS_XN = 0;
constexpr size_t WS_Q = WS_XN + (size_t)S_ * D_ * 2;
constexpr size_t WS_KV = WS_Q + (size_t)S_ * D_ * 2;
constexpr size_t WS_KMAX = WS_KV + 8 * KV_HSG;
constexpr size_t WS_WP = WS_KMAX + 1024;
constexpr size_t WS_H = WS_WP + (size_t)4 * 162 * 9216;
constexpr size_t WS_P = WS_H + (size_t)64 * NBLK * 1024;

DI unsigned cvtpk(float lo, float hi) {
  typedef __bf16 b2 __attribute__((ext_vector_type(2)));
  typedef float f2 __attribute__((ext_vector_type(2)));
  f2 v = {lo, hi};
  return __builtin_bit_cast(unsigned, __builtin_convertvector(v, b2));
}
DI float bflo(unsigned u) { return __uint_as_float(u << 16); }
DI float bfhi(unsigned u) { return __uint_as_float(u & 0xffff0000u); }
DI void glds16(const void* sbase, unsigned voff, unsigned lds_addr) {
  asm volatile("s_mov_b32 m0, %0\n\ts_nop 0\n\tglobal_load_lds_dwordx4 %1, %2" ::"s"(lds_addr), "v"(voff), "s"(sbase) : "memory");
}
DI void store16_wt(void* p, u32x4 v) {
  asm volatile("global_store_dwordx4 %0, %1, off sc1\n\ts_nop 1" ::"v"(p), "v"(v) : "memory");
}

DI bf16x4 trread(lds_cptr p) {
  return __builtin_amdgcn_ds_read_tr16_b64_v4i16((__attribute__((address_space(3))) bf16x4*)p);
}
DI bf16x8 cat(bf16x4 a, bf16x4 b) { return __builtin_shufflevector(a, b, 0, 1, 2, 3, 4, 5, 6, 7); }
#define MFMA32(a, b, c) __builtin_amdgcn_mfma_f32_32x32x16_bf16((a), (b), (c), 0, 0, 0)
#define MFMA32K8(a, b, c) __builtin_amdgcn_mfma_f32_32x32x8bf16_1k((a), (b), (c), 0, 0, 0)
#define MFMA16(a, b, c) __builtin_amdgcn_mfma_f32_16x16x32_bf16((a), (b), (c), 0, 0, 0)
#define MFMA16A(c, a, b) asm volatile("v_mfma_f32_16x16x32_bf16 %0, %1, %2, %0" : "+v"(c) : "v"(a), "v"(b))
#define MFMA_DRAIN() asm volatile("s_nop 7\n\ts_nop 7" ::: "memory")
#define SBAR() __builtin_amdgcn_sched_barrier(0)
#define PIN(x) asm volatile("" : "+v"(x))
#define LDS128(p) (*(const __attribute__((address_space(3))) bf16x8*)(p))
#define LDS64(p) (*(const __attribute__((address_space(3))) bf16x4*)(p))
#define WAIT_VM0() __builtin_amdgcn_s_waitcnt(0x0F70)
#define BARRIER_LGKM() asm volatile("s_waitcnt lgkmcnt(0)\n\ts_barrier" ::: "memory")
#define STAMP_AT(kid, slot) do { if ((threadIdx.x & 63) == 0) { unsigned long long* sp_ = (unsigned long long*)(ws + (200u << 20)) + ((size_t)(kid) * 1024 * 8 + blockIdx.x * 8 + (threadIdx.x >> 6)) * 16 + (slot) * 2; \
    sp_[0] = __builtin_amdgcn_s_memrealtime(); sp_[1] = __builtin_amdgcn_s_memtime(); } } while (0)
#define STAMP_DECL unsigned long long stc_[8] = {}, str_[8] = {}
#define STAMP_R(slot) do { stc_[slot] = __builtin_amdgcn_s_memtime(); str_[slot] = __builtin_amdgcn_s_memrealtime(); } while (0)
#define STAMP_FLUSH(kid, lo, hi) do { if ((threadIdx.x & 63) == 0) { unsigned long long* sp_ = (unsigned long long*)(ws + (200u << 20)) + ((size_t)(kid) * 1024 * 8 + blockIdx.x * 8 + (threadIdx.x >> 6)) * 16; \
    _Pragma("unroll") for (int i_ = lo; i_ <= hi; ++i_) { sp_[2 * i_] = str_[i_]; sp_[2 * i_ + 1] = stc_[i_]; } } } while (0)

constexpr int K1_PK = 144 * 800, K1_XT = K1_PK + 3 * 9216, K1_LDS = K1_XT + 4 * 4352;
typedef float f32x2 __attribute__((ext_vector_type(2)));
DI float quad_sum(float v) {
  const auto a = __builtin_amdgcn_permlane32_swap(__float_as_uint(v), __float_as_uint(v), false, false);
  const float s = __uint_as_float(a[0]) + __uint_as_float(a[1]);
  const auto b = __builtin_amdgcn_permlane16_swap(__float_as_uint(s), __float_as_uint(s), false, false);
  return __uint_as_float(b[0]) + __uint_as_float(b[1]);
}
#define K1ROT 1
__global__ __launch_bounds__(512) void k1_ln_qkv(const unsigned short* __restrict__ x, const unsigned short* __restrict__ wq,
                                                 const unsigned short* __restrict__ wk, const unsigned short* __restrict__ wv,
                                                 const unsigned short* __restrict__ wo, const unsigned short* __restrict__ w2, char* __restrict__ ws) {
  extern __shared__ __attribute__((aligned(16))) char smem[];
  const int tid = threadIdx.x, lane = tid & 63;
  const int w = __builtin_amdgcn_readfirstlane(tid >> 6);
  const int g = (blockIdx.x >> 3) & 7, tg = (blockIdx.x & 7) * 4 + (blockIdx.x >> 6);
  const unsigned lds0 = (unsigned)(uintptr_t)smem;
  const int c = lane & 15, q = lane >> 4;
  STAMP_DECL;
  STAMP_R(0);
  if (w >= 4) {
    asm volatile("s_barrier" ::: "memory");
    if (g == 7) {
      for (int k = tid - 256; k < 576; k += 256) {
        const u32x4* a = (const u32x4*)(wq + (size_t)k * D_ + 560);
        const u32x4 a0 = a[0], a1 = a[1];
        const unsigned* kp = (const unsigned*)(wk + k * 18);
        const unsigned* vp = (const unsigned*)(wv + k * 18);
        const u32x4 k0 = *(const u32x4_a4*)kp, k1 = *(const u32x4_a4*)(kp + 4);
        const unsigned k2 = kp[8];
        const u32x4 v0 = *(const u32x4_a4*)vp, v1 = *(const u32x4_a4*)(vp + 4);
        const unsigned v2 = vp[8];
        char* d = smem + ((k >> 2) ^ ((k >> 3) & 1)) * 800 + (k & 3) * 192;
        *(u32x4*)(d) = a0; *(u32x4*)(d + 16) = a1;
        *(u32x4*)(d + 32) = k0; *(u32x4*)(d + 48) = k1; *(unsigned*)(d + 64) = k2;
        *(u32x4*)(d + 96) = v0; *(u32x4*)(d + 112) = v1; *(unsigned*)(d + 128) = v2;
      }
      BARRIER_LGKM();
      return;
    }
    if (lane < 48) {
      const unsigned row = lane / 12u, voff = row * 1152u + (lane - row * 12u) * 16u;
      const char* base = (const char*)wq + ((160 * g) & ~63) + ((w & 3) ^ ((w >> 1) & 1)) * 4608;
      int u = K1ROT * (tg & 3) * 9;
#pragma unroll 4
      for (int i = 0; i < 36; ++i) {
        glds16(base + u * 18432, voff, lds0 + (w & 3) * 800 + u * 3200);
        u = u == 35 ? 0 : u + 1;
      }
    }
    STAMP_R(4);
    const int job = (tg * 7 + g) * 3 + w - 5;
    if (w == 4 || job >= 648) {
      asm volatile("s_waitcnt vmcnt(0)\n\ts_barrier" ::: "memory");
      return;
    }
    const int j = job >> 2;
    const unsigned ra = j < 144 ? 16 : j < 160 ? 18 : 144, rb = j < 160 ? 2 : 18, rc = j < 144 ? 32 * j : j < 160 ? 36 * (j - 144) : 288 * (j - 160) + 16;
    const char* base = (const char*)(j < 144 ? w2 : wo) + (size_t)rc * 1152 + (job & 3) * 288;
#pragma unroll
    for (int v = 0; v < 9; ++v) {
      const unsigned ch = v * 64 + lane, row = ch / 18u, cc = ch - row * 18u;
      const unsigned src = ra * (row >> 4) + rb * ((row & 15) >> 1) + (row & 1);
      glds16(base, src * 1152u + cc * 16u, lds0 + K1_PK + (w - 5) * 9216 + v * 1024);
    }
    STAMP_R(5);
    asm volatile("s_waitcnt vmcnt(9)\n\ts_barrier\n\ts_waitcnt vmcnt(0)" ::: "memory");
    STAMP_R(2);
    const lds_cptr pk = (lds_cptr)smem + K1_PK + (w - 5) * 9216 + (16 * (q >> 1) + 4 * (q & 1) + (c >> 2)) * 288;
    char* wp = ws + WS_WP + (size_t)((job & 3) * 162 + j) * 9216 + lane * 16;
#pragma unroll
    for (int b = 0; b < 9; ++b) {
      const lds_cptr a = pk + (b < 8 ? 64 * (b >> 1) + 8 * (b & 1) + 16 * (c & 3) : 256 + 16 * (c & 1) + 8 * ((c >> 1) & 1));
      store16_wt(wp + b * 1024, __builtin_bit_cast(u32x4, cat(trread(a), trread(a + 8 * 288))));
    }
    STAMP_R(3);
    STAMP_FLUSH(0, 0, 7);
    return;
  }
  const int tok = tg * 64 + w * 16 + c;
  u32x4 xr[18];
  {
    const char* xp = (const char*)x + (tg * 64 + w * 16 + (c & 12)) * 1152 + q * 64 + (c & 3) * 16;
#pragma unroll
    for (int i = 0; i < 16; ++i) xr[i] = *(const u32x4*)(xp + (i & 3) * 1152 + (i >> 2) * 256);
#pragma unroll
    for (int i = 16; i < 18; ++i) xr[i] = *(const u32x4*)((const char*)x + tok * 1152 + i * 64 + q * 16);
    asm volatile("s_barrier" ::: "memory");
    STAMP_R(6);
    typedef __attribute__((address_space(3))) u32x4* lds_v4;
    const lds_ptr tw = (lds_ptr)smem + K1_XT + w * 4352 + (c & 12) * 272 + q * 64 + (c & 3) * 16, tr = (lds_ptr)smem + K1_XT + w * 4352 + c * 272 + q * 16;
#pragma unroll
    for (int gq = 0; gq < 4; ++gq) {
#pragma unroll
      for (int i = 0; i < 4; ++i) *(lds_v4)(tw + i * 272) = xr[4 * gq + i];
      asm volatile("" ::: "memory");
#pragma unroll
      for (int i = 0; i < 4; ++i) xr[4 * gq + i] = *(lds_v4)(tr + i * 64);
      asm volatile("" ::: "memory");
    }
    STAMP_R(4);
  }
  f32x2 d[72];
  f32x2 s2[2] = {{0.f, 0.f}, {0.f, 0.f}};
#pragma unroll
  for (int i = 0; i < 72; ++i) { d[i] = f32x2{bflo(xr[i >> 2][i & 3]), bfhi(xr[i >> 2][i & 3])}; s2[i & 1] += d[i]; }
  const float mean = quad_sum((s2[0][0] + s2[0][1]) + (s2[1][0] + s2[1][1])) * (1.f / 576.f);
  STAMP_R(5);
  f32x2 v2[2] = {{0.f, 0.f}, {0.f, 0.f}};
#pragma unroll
  for (int i = 0; i < 72; ++i) { d[i] -= mean; v2[i & 1] += d[i] * d[i]; }
  const float rstd = 1.f / sqrtf(quad_sum((v2[0][0] + v2[0][1]) + (v2[1][0] + v2[1][1])) * (1.f / 576.f) + 1e-5f);
#pragma unroll
  for (int i = 0; i < 72; ++i) { const f32x2 y = d[i] * rstd; xr[i >> 2][i & 3] = cvtpk(y[0], y[1]); }
  {
    char* xn = ws + WS_XN + (size_t)(tok >> 5) * 36864 + (w & 1) * 1024 + lane * 16;
#pragma unroll
    for (int i = 0; i < 18; ++i)
      if ((i & 7) == g) store16_wt(xn + i * 2048, xr[i]);
  }
  STAMP_R(1);
  asm volatile("s_barrier" ::: "memory");
  STAMP_R(2);
  f32x4 acc[5];
#pragma unroll
  for (int b = 0; b < 5; ++b) acc[b] = f32x4{0.f, 0.f, 0.f, 0.f};
  const int co = (c >> 2) * 192 + 8 * (c & 3) + (g < 7 ? (g & 1) * 32 : 0);
  const lds_cptr p0 = (lds_cptr)smem + (2 * q + (q & 1)) * 800 + co, p1 = (lds_cptr)smem + (2 * q + 1 - (q & 1)) * 800 + co;
  const lds_cptr p2 = p0 + 57600, p3 = p1 + 57600;
  bf16x4 al[10], ah[10];
#define K1_RD(n) do { al[(n) % 10] = trread(((n) < 45 ? p0 : p2) + ((n) % 45 / 5) * 6400 + ((n) % 5) * 32); \
                      ah[(n) % 10] = trread(((n) < 45 ? p1 : p3) + ((n) % 45 / 5) * 6400 + ((n) % 5) * 32); } while (0)
#pragma unroll
  for (int n = 0; n < 9; ++n) K1_RD(n);
  if (g < 7) {
#pragma unroll
    for (int n = 0; n < 90; ++n) {
      if (n + 9 < 90) K1_RD(n + 9);
      acc[n % 5] = MFMA16(cat(al[n % 10], ah[n % 10]), __builtin_bit_cast(bf16x8, xr[n / 5]), acc[n % 5]);
      SBAR();
    }
  } else {
#pragma unroll
    for (int n = 0; n < 90; ++n) {
      if (n + 9 < 90) K1_RD(n + 9);
      if (n % 5 < 3) acc[n % 5] = MFMA16(cat(al[n % 10], ah[n % 10]), __builtin_bit_cast(bf16x8, xr[n / 5]), acc[n % 5]);
      else acc[n % 5] = MFMA16(__builtin_bit_cast(bf16x8, xr[n / 5]), cat(al[n % 10], ah[n % 10]), acc[n % 5]);
      SBAR();
    }
  }
#undef K1_RD
  STAMP_R(7);
  constexpr float QSCALE = 0.23570226039551584f * 1.4426950408889634f;
  char* qp = ws + WS_Q + ((size_t)tok * D_ + 80 * g + 4 * q) * 2;
#define QST(b) *(u32x2*)(qp + (b) * 32) = u32x2{cvtpk(acc[b][0] * QSCALE, acc[b][1] * QSCALE), cvtpk(acc[b][2] * QSCALE, acc[b][3] * QSCALE)}
  QST(0);
  if (g < 7) {
    QST(1); QST(2); QST(3); QST(4);
  } else {
    char* kv = ws + WS_KV + (tok >> 8) * KV_HSG;
    const int kk = tok & 255;
    const u32x2 kA = {cvtpk(acc[1][0], acc[1][1]), cvtpk(acc[1][2], acc[1][3])};
    const unsigned kB = cvtpk(acc[2][0], acc[2][1]);
    *(u32x2*)(kv + (kk >> 5) * 1024 + ((q >> 1) * 32 + (kk & 31)) * 16 + (q & 1) * 8) = kA;
    if (q == 0) *(u32x2*)(kv + KV_K2 + kk * 8) = u32x2{kB, 0x3F803F80u};
    const int t0 = (tg * 64 + w * 16) & 255;
    char* vp = kv + KV_V + (t0 >> 4) * 624 + (q & 1) * 304 + (q >> 1) * 8;
    *(u32x2*)(vp + c * 16) = u32x2{cvtpk(acc[3][0], acc[3][1]), cvtpk(acc[3][2], acc[3][3])};
    if (c < 2) *(u32x2*)(vp + (16 + c) * 16) = u32x2{cvtpk(acc[4][0], acc[4][1]), cvtpk(acc[4][2], acc[4][3])};
    if (c == 2) *(u32x2*)(vp + 18 * 16) = u32x2{0x3F803F80u, 0x3F803F80u};
    if (c == 3 && (q & 1)) *(u32x2*)(vp + 19 * 16) = u32x2{0u, 0u};
    float n2 = bflo(kA[0]) * bflo(kA[0]) + bfhi(kA[0]) * bfhi(kA[0]) + bflo(kA[1]) * bflo(kA[1]) + bfhi(kA[1]) * bfhi(kA[1]);
    if (q == 0) n2 += bflo(kB) * bflo(kB) + bfhi(kB) * bfhi(kB);
    n2 = quad_sum(n2);
    n2 = fmaxf(n2, __shfl_xor(n2, 1)); n2 = fmaxf(n2, __shfl_xor(n2, 2));
    n2 = fmaxf(n2, __shfl_xor(n2, 4)); n2 = fmaxf(n2, __shfl_xor(n2, 8));
    if (lane == 0) ((float*)(ws + WS_KMAX))[tg * 4 + w] = n2;
  }
#undef QST
  STAMP_R(3);
  STAMP_FLUSH(0, 0, 7);
}

#define HSTORE(p, v) store16_wt(p, v)
constexpr int K2_FLAG = 4 * KV_HS;
constexpr int K2_LDS = K2_FLAG + 16;

#define KV_LOADS "s_mov_b32 m0, %0\n\ts_nop 0\n\t" \
               "global_load_lds_dwordx4 %1, %3\n\tglobal_load_lds_dwordx4 %1, %3 offset:1024\n\t" \
               "global_load_lds_dwordx4 %1, %3 offset:2048\n\tglobal_load_lds_dwordx4 %1, %3 offset:3072\n\t" \
               "s_add_u32 m0, %0, 0x1000\n\ts_mov_b32 exec_hi, %4\n\tglobal_load_lds_dwordx4 %2, %3\n\ts_mov_b32 exec_hi, -1\n"
DI void kv_issue(const char* src, unsigned va, unsigned vb, unsigned dst, int m5hi) {
  asm volatile(KV_LOADS ::"s"(dst), "v"(va), "v"(vb), "s"(src), "s"(m5hi) : "memory", "scc");
}
#define PFHS "6"
DI void kv_turn(int hs, const char* src, unsigned va, unsigned vb, unsigned dst, int m5hi, char* box, unsigned voff, u32x4 ones) {
  asm volatile("s_cmp_lt_u32 %5, 5\n\ts_cbranch_scc0 1f\n\ts_waitcnt vmcnt(5)\n\ts_barrier\n\t" KV_LOADS "\ts_branch 2f\n"
               "1:\n\ts_waitcnt vmcnt(0)\n\ts_barrier\n\ts_cmp_lg_u32 %5, " PFHS "\n\ts_cbranch_scc1 2f\n\t"
               "global_store_dwordx4 %7, %8, %6 offset:-3072 sc1\n\tglobal_store_dwordx4 %7, %8, %6 offset:-2048 sc1\n\t"
               "global_store_dwordx4 %7, %8, %6 offset:-1024 sc1\n\tglobal_store_dwordx4 %7, %8, %6 sc1\n\t"
               "global_store_dwordx4 %7, %8, %6 offset:1024 sc1\n\tglobal_store_dwordx4 %7, %8, %6 offset:2048 sc1\n\t"
               "global_store_dwordx4 %7, %8, %6 offset:3072 sc1\n"
               "2:"
               ::"s"(dst), "v"(va), "v"(vb), "s"(src), "s"(m5hi), "s"(hs), "s"(box), "v"(voff), "v"(ones) : "memory", "scc");
}
#undef KV_LOADS
DI bf16x4 q_tail(unsigned q2, float m, int h) {
  const float m1 = bflo(cvtpk(m, 0.f)), r1 = m - m1, m2 = bflo(cvtpk(r1, 0.f)), m3 = r1 - m2;
  return __builtin_bit_cast(bf16x4, h ? u32x2{0u, cvtpk(-m3, 0.f)} : u32x2{q2, cvtpk(-m1, -m2)});
}

DI void kv_first(char* smem, const char* kvg, int w, int lane) {
  const unsigned va = w * 5120 + lane * 16;
#pragma unroll
  for (int i = 0; i < 3; ++i) kv_issue(kvg + i * KV_HSG, va, va + 4096, (unsigned)(uintptr_t)smem + w * 5120 + i * KV_HS, w == 3 ? 0xFFFF : -1);
}

#define APRIO_HI 2
#define APRIO_LO 0
DI void attn_main(char* ws, char* smem, const char* kvg, char* box, int w, int lane, bf16x8 qa1, bf16x4 qa2, bf16x8 qb1, bf16x4 qb2, f32x16& oa, f32x16& ob) {
  const unsigned lds0 = (unsigned)(uintptr_t)smem;
  const int r = lane & 31, h = lane >> 5;
  const unsigned va = w * 5120 + lane * 16, vb = va + 4096, dst = lds0 + w * 5120;
  const int m5 = w == 3 ? 0xFFFF : -1;
  const int ok1 = lane * 16, ok2 = KV_K2 + r * 8, ov = KV_V + (r < 19 ? h * 304 + r * 16 : 608);
  const f32x16 zero = {};
  u32x4 ones = {~0u, ~0u, ~0u, ~0u};
  PIN(ones);
  STAMP_DECL;
  STAMP_R(2);
  asm volatile("s_waitcnt vmcnt(10)\n\ts_barrier" ::: "memory");
  STAMP_R(3);
  bf16x8 k1 = LDS128((lds_cptr)smem + ok1), v0 = LDS128((lds_cptr)smem + ov), v1 = {};
  bf16x4 k2 = LDS64((lds_cptr)smem + ok2);
  f32x16 sa = MFMA32(k1, qa1, MFMA32K8(k2, qa2, zero)), sb;
  u32x4 p0, p1 = {0u, 0u, 0u, 0u};
  float e2 = 0.f, e3 = 0.f;
#define SLOT(MF, RD, NEXT, S, i, PP, jp, P, j) do { \
    MF; SBAR(); __builtin_amdgcn_s_setprio(APRIO_LO); SBAR(); \
    float e0 = __builtin_amdgcn_exp2f(S[i]); PIN(e0); SBAR(); RD; SBAR(); \
    float e1 = __builtin_amdgcn_exp2f(S[i + 1]); PIN(e1); SBAR(); \
    PP[jp] = cvtpk(e2, e3); PIN(PP); SBAR(); \
    e2 = __builtin_amdgcn_exp2f(S[i + 2]); PIN(e2); SBAR(); PIN(NEXT); SBAR(); \
    e3 = __builtin_amdgcn_exp2f(S[i + 3]); PIN(e3); SBAR(); __builtin_amdgcn_s_setprio(APRIO_HI); SBAR(); \
    P[j] = cvtpk(e0, e1); PIN(P); SBAR(); } while (0)
#define PB(x) __builtin_bit_cast(bf16x8, x)
  for (int hs = 0; hs < 8; ++hs) {
    const lds_cptr cur = (lds_cptr)smem + (hs & 3) * KV_HS, nxt = (lds_cptr)smem + ((hs + 1) & 3) * KV_HS;
    const lds_cptr ck1 = cur + ok1, ck2 = cur + ok2, cv = cur + ov, nk1 = nxt + ok1, nk2 = nxt + ok2, nv = nxt + ov;
#pragma unroll
    for (int t = 0; t < 8; ++t) {
      if (t == 7) kv_turn(hs, kvg + ((hs + 3) & 7) * KV_HSG, va, vb, dst + ((hs + 3) & 3) * KV_HS, m5, box, ok1, ones);
      SBAR();
      SLOT(sb = MFMA32K8(k2, qb2, zero), k2 = t < 7 ? LDS64(ck2 + (t + 1) * 256) : LDS64(nk2), e0, sa, 0, p1, 3, p0, 0);
      SLOT(ob = MFMA32(v1, PB(p1), ob), v1 = LDS128(cv + (2 * t + 1) * 624), e0, sa, 4, p0, 1, p0, 2);
      SLOT(sb = MFMA32(k1, qb1, sb), k1 = t < 7 ? LDS128(ck1 + (t + 1) * 1024) : LDS128(nk1), v0, sa, 8, p0, 3, p1, 0);
      SLOT(oa = MFMA32(v0, PB(p0), oa), , k2, sa, 12, p1, 1, p1, 2);
      SLOT(sa = MFMA32K8(k2, qa2, zero), , v1, sb, 0, p1, 3, p0, 0);
      SLOT(oa = MFMA32(v1, PB(p1), oa), , k1, sb, 4, p0, 1, p0, 2);
      SLOT(sa = MFMA32(k1, qa1, sa), , e0, sb, 8, p0, 3, p1, 0);
      SLOT(ob = MFMA32(v0, PB(p0), ob), v0 = t < 7 ? LDS128(cv + (2 * t + 2) * 624) : LDS128(nv), e0, sb, 12, p1, 1, p1, 2);
    }
  }
#undef SLOT
  STAMP_R(4);
  STAMP_FLUSH(1, 2, 4);
  p1[3] = cvtpk(e2, e3);
  ob = MFMA32(v1, PB(p1), ob);
#undef PB
  asm volatile("s_waitcnt vmcnt(0) lgkmcnt(0)\n\ts_barrier" ::: "memory");
}

DI void attn_max(char* smem, const char* kvg, int w, int lane, bf16x8 qa1, bf16x4 qa2, bf16x8 qb1, bf16x4 qb2, float& ma, float& mb) {
  const unsigned lds0 = (unsigned)(uintptr_t)smem;
  const unsigned va = w * 5120 + lane * 16;
  const f32x16 zero = {};
  for (int hs = 0; hs < 8; ++hs) {
    kv_issue(kvg + hs * KV_HSG, va, va + 4096, lds0 + w * 5120, w == 3 ? 0xFFFF : -1);
    asm volatile("s_waitcnt vmcnt(0)\n\ts_barrier" ::: "memory");
#pragma unroll 1
    for (int t = 0; t < 8; ++t) {
      const bf16x8 k1 = LDS128((lds_cptr)smem + lane * 16 + t * 1024);
      const bf16x4 k2 = LDS64((lds_cptr)smem + KV_K2 + (lane & 31) * 8 + t * 256);
      const f32x16 sa = MFMA32(k1, qa1, MFMA32K8(k2, qa2, zero)), sb = MFMA32(k1, qb1, MFMA32K8(k2, qb2, zero));
#pragma unroll
      for (int i = 0; i < 16; ++i) { ma = fmaxf(ma, sa[i]); mb = fmaxf(mb, sb[i]); }
    }
    BARRIER_LGKM();
  }
  ma = fmaxf(ma, __shfl_xor(ma, 32));
  mb = fmaxf(mb, __shfl_xor(mb, 32));
}

DI void attn_store(char* hp, int hd, int lane, const f32x16& o, float l) {
  const float inv = 1.f / l;
  HSTORE(hp + (288 + hd) * 1024 + lane * 16,
         (u32x4{cvtpk(o[0] * inv, o[1] * inv), cvtpk(o[2] * inv, o[3] * inv), cvtpk(o[4] * inv, o[5] * inv), cvtpk(o[6] * inv, o[7] * inv)}));
  if (lane < 32) {
    const int p = 2 * (hd & 7);
    *(unsigned*)(hp + (320 + (hd >> 3)) * 1024 + (((p >> 2) & 1) * 32 + lane) * 16 + 2 * (4 * (p >> 3) + (p & 3))) = cvtpk(o[8] * inv, o[9] * inv);
  }
}

DI float half_sum(float v) {
  const auto t = __builtin_amdgcn_permlane32_swap(__float_as_uint(v), __float_as_uint(v), false, false);
  return __uint_as_float(t[0]) + __uint_as_float(t[1]);
}
DI void attn_unit(char* smem, char* ws, int u, int w, int lane) {
  const char* kvg = ws + WS_KV;
  kv_first(smem, kvg, w, lane);
  const int gw = u * 4 + w, hd = (gw & 15) * 2, tb = gw >> 4;
  const int r = lane & 31, h = lane >> 5;
  const char* qrow = ws + WS_Q + ((size_t)(tb * 32 + r) * D_ + 18 * hd) * 2;
  const u32x4 qa = *(const u32x4_a4*)(qrow + 16 * h), qb = *(const u32x4_a4*)(qrow + 36 + 16 * h);
  const unsigned qa2 = *(const unsigned*)(qrow + 32), qb2 = *(const unsigned*)(qrow + 68);
  const u32x2 kx = *(const u32x2*)(ws + WS_KMAX + lane * 8);
  unsigned ku = max(kx[0], kx[1]);
  ku = max(ku, (unsigned)__builtin_amdgcn_update_dpp(0, (int)ku, 0xB1, 0xF, 0xF, true));
  ku = max(ku, (unsigned)__builtin_amdgcn_update_dpp(0, (int)ku, 0x4E, 0xF, 0xF, true));
  ku = max(ku, (unsigned)__builtin_amdgcn_update_dpp(0, (int)ku, 0x141, 0xF, 0xF, true));
  ku = max(ku, (unsigned)__builtin_amdgcn_update_dpp(0, (int)ku, 0x140, 0xF, 0xF, true));
  const float km = __uint_as_float(max(max((unsigned)__builtin_amdgcn_readlane(ku, 0), (unsigned)__builtin_amdgcn_readlane(ku, 16)),
                                       max((unsigned)__builtin_amdgcn_readlane(ku, 32), (unsigned)__builtin_amdgcn_readlane(ku, 48))));
  float na = 0.f, nb = 0.f;
#pragma unroll
  for (int j = 0; j < 4; ++j) {
    na += bflo(qa[j]) * bflo(qa[j]) + bfhi(qa[j]) * bfhi(qa[j]);
    nb += bflo(qb[j]) * bflo(qb[j]) + bfhi(qb[j]) * bfhi(qb[j]);
  }
  na = half_sum(na) + bflo(qa2) * bflo(qa2) + bfhi(qa2) * bfhi(qa2);
  nb = half_sum(nb) + bflo(qb2) * bflo(qb2) + bfhi(qb2) * bfhi(qb2);
  float ma = sqrtf(na * km) * 1.001f, mb = sqrtf(nb * km) * 1.001f;
  volatile __attribute__((address_space(3))) int* flag = (volatile __attribute__((address_space(3))) int*)((lds_ptr)smem + K2_FLAG);
  if (w == 0 && lane == 0) *flag = 0;
  const bf16x8 qa1 = __builtin_bit_cast(bf16x8, qa), qb1 = __builtin_bit_cast(bf16x8, qb);
  f32x16 oa, ob;
  float la, lb;
  for (int attempt = 0;; ++attempt) {
#pragma unroll
    for (int i = 0; i < 16; ++i) { oa[i] = 0.f; ob[i] = 0.f; }
    attn_main(ws, smem, kvg, ws + WS_P + (size_t)gw * 7168 + 3072, w, lane, qa1, q_tail(qa2, ma, h), qb1, q_tail(qb2, mb, h), oa, ob);
    la = __shfl(oa[10], r); lb = __shfl(ob[10], r);
    if (attempt) break;
    if (!(la >= 8.2718061e-25f && lb >= 8.2718061e-25f)) *flag = 1;
    BARRIER_LGKM();
    const int bad = *flag;
    if (!__builtin_amdgcn_readfirstlane(bad)) break;
    ma = -INFINITY; mb = -INFINITY;
    attn_max(smem, kvg, w, lane, qa1, q_tail(qa2, 0.f, h), qb1, q_tail(qb2, 0.f, h), ma, mb);
    kv_first(smem, kvg, w, lane);
  }
  char* hp = ws + WS_H + (size_t)tb * NBLK * 1024;
  attn_store(hp, hd, lane, oa, la);
  attn_store(hp, hd + 1, lane, ob, lb);
}

DI float silu_mul(float g, float u) {
  const float e = __builtin_amdgcn_exp2f(g * -1.4426950408889634f);
  return g * __builtin_amdgcn_rcpf(1.f + e) * u;
}

constexpr int G1_W3 = 9216, G1_SLOT = G1_W3 + 9216;
DI void glds16h(const void* sbase, unsigned voff, unsigned lds_addr) {
  asm volatile("s_mov_b32 m0, %0\n\ts_mov_b32 exec_hi, 0\n\tglobal_load_lds_dwordx4 %1, %2\n\ts_mov_b32 exec_hi, -1" ::"s"(lds_addr), "v"(voff), "s"(sbase) : "memory");
}
template <int OFF>
DI void gload16(u32x4& a, const void* sbase, unsigned voff) {
  asm volatile("global_load_dwordx4 %0, %1, %2 offset:%3" : "+v"(a) : "v"(voff), "s"(sbase), "i"(OFF) : "memory");
}

DI void gemm1_unit(char* smem, char* ws, const unsigned short* w1, const unsigned short* w3, const unsigned short* w2, int u, int w, int lane) {
  const unsigned lds0 = (unsigned)(uintptr_t)smem;
  const int j = u >> 3, ct = (u & 7) * 4 + (j & 3), rt = j >> 2;
  const char* wsrc = (const char*)((w >> 1) ? w3 : w1) + (w & 1) * 16 * 9216 + ct * 288;
  const unsigned wdst = lds0 + (w >> 1) * G1_W3 + (w & 1) * 4608;
  unsigned woff[5];
#pragma unroll
  for (int i = 0; i < 5; ++i) { const unsigned ch = i * 64 + (i < 4 ? lane : lane & 31), row = ch / 18u; woff[i] = (row ^ ((row >> 1) & 4u)) * 9216u + (ch - row * 18u) * 16u; }
  const char* xsrc = ws + WS_XN + (size_t)(rt * 4 + w) * 36864;
  const unsigned xoff = lane * 16;
  int sw = rt * 9 / 4, sx = sw;
  u32x4 xf[3][2];
#pragma unroll
  for (int i = 0; i < 3; ++i) { xf[i][0] = u32x4{0, 0, 0, 0}; xf[i][1] = u32x4{0, 0, 0, 0}; }
#define G1_WP(slot, i) do { if ((i) < 4) glds16(wsrc + (size_t)sw * 294912, woff[i], wdst + (slot) * G1_SLOT + (i) * 1024); \
    else { glds16h(wsrc + (size_t)sw * 294912, woff[4], wdst + (slot) * G1_SLOT + 4096); sw = sw == 17 ? 0 : sw + 1; } } while (0)
#define G1_XP(set, i) do { if ((i) == 0) gload16<0>(xf[set][0], xsrc + sx * 2048, xoff); \
    else { gload16<1024>(xf[set][1], xsrc + sx * 2048, xoff); sx = sx == 17 ? 0 : sx + 1; } } while (0)
#pragma unroll
  for (int s = 0; s < 3; ++s) {
#pragma unroll
    for (int i = 0; i < 5; ++i) G1_WP(s, i);
    if (s) { G1_XP(s - 1, 0); G1_XP(s - 1, 1); }
  }
  const int c = lane & 15, q = lane >> 4;
  const int aoff0 = (8 * q + 4 * (q & 1) + (c >> 2)) * 288 + 8 * (c & 3), aoff1 = (8 * q + 4 * ((q & 1) ^ 1) + (c >> 2)) * 288 + 8 * (c & 3);
#define G1_RD(p, i) trread((p) + ((i) / 9) * G1_W3 + ((i) % 9) * 32)
  bf16x4 al[6], ah[6];
  STAMP_DECL;
  STAMP_R(2);
  asm volatile("s_waitcnt vmcnt(14)\n\ts_barrier" ::: "memory");
  STAMP_R(3);
#pragma unroll
  for (int i = 0; i < 6; ++i) { al[i] = G1_RD((lds_cptr)smem + aoff0, i); if (i < 5) ah[i] = G1_RD((lds_cptr)smem + aoff1, i); }
  int g = 0;
  f32x4 acc[36];
#define G1_STAGE(s, VM, LW, LX) do { \
    asm volatile("s_waitcnt vmcnt(" #VM ")\n\ts_barrier" : "+v"(xf[s][0]), "+v"(xf[s][1]) :: "memory"); \
    const lds_cptr pc = (lds_cptr)smem + (g & 3) * G1_SLOT, pn = (lds_cptr)smem + ((g + 1) & 3) * G1_SLOT; \
    const lds_cptr pc0 = pc + aoff0, pc1 = pc + aoff1, pn0 = pn + aoff0, pn1 = pn + aoff1; \
    SBAR(); \
    _Pragma("unroll") for (int i = 0; i < 18; ++i) { \
      MFMA16A(acc[2 * i], cat(al[i % 6], ah[i % 6]), xf[s][0]); \
      SBAR(); \
      ah[(i + 5) % 6] = i < 13 ? G1_RD(pc1, i + 5) : G1_RD(pn1, i - 13); \
      if (LW && (i & 1) && i < 10) G1_WP((g + 3) & 3, i >> 1); \
      if (LX && i == 11) { if (g == 16) xsrc += 32 * 36864; G1_XP((s + 2) % 3, 0); } \
      if (LX && i == 13) G1_XP((s + 2) % 3, 1); \
      SBAR(); \
      MFMA16A(acc[2 * i + 1], cat(al[i % 6], ah[i % 6]), xf[s][1]); \
      SBAR(); \
      al[i % 6] = i < 12 ? G1_RD(pc0, i + 6) : G1_RD(pn0, i - 12); \
      SBAR(); \
    } \
    ++g; } while (0)
  for (int un = 0; un < 2; ++un) {
#pragma unroll
    for (int b = 0; b < 36; ++b) acc[b] = f32x4{0.f, 0.f, 0.f, 0.f};
    for (int trip = un; trip < 6; ++trip) { G1_STAGE(0, 7, true, true); G1_STAGE(1, 7, true, true); G1_STAGE(2, 7, true, true); }
    if (un) { G1_STAGE(0, 7, false, true); G1_STAGE(1, 2, false, false); G1_STAGE(2, 0, false, false); }
    MFMA_DRAIN();
    if (un == 0) STAMP_R(4); else STAMP_R(6);
    char* hp = ws + WS_H + ((size_t)(rt * 4 + un * 32 + w) * NBLK + ct * 9) * 1024 + ((q & 1) * 32 + (q >> 1) * 16 + c) * 16;
#pragma unroll
    for (int b = 0; b < 9; ++b) {
      PIN(acc[2 * b]); PIN(acc[2 * b + 1]); PIN(acc[18 + 2 * b]); PIN(acc[19 + 2 * b]);
      unsigned t[2][2];
#pragma unroll
      for (int i = 0; i < 4; ++i) {
        const f32x4 &ga = acc[2 * b + (i >> 1)], &ua = acc[18 + 2 * b + (i >> 1)];
        t[i >> 1][i & 1] = cvtpk(silu_mul(ga[2 * (i & 1)], ua[2 * (i & 1)]), silu_mul(ga[2 * (i & 1) + 1], ua[2 * (i & 1) + 1]));
      }
      const auto s0 = __builtin_amdgcn_permlane32_swap(t[0][0], t[1][0], false, false), s1 = __builtin_amdgcn_permlane32_swap(t[0][1], t[1][1], false, false);
      HSTORE(hp + b * 1024, (u32x4{s0[0], s1[0], s0[1], s1[1]}));
    }
    if (un == 0) STAMP_R(5);
  }
  STAMP_FLUSH(K2MODE ? 4 : 1, 2, 6);
#undef G1_WP
#undef G1_XP
#undef G1_RD
#undef G1_STAGE
}

template <int MODE>
__global__ __launch_bounds__(256, 2) void k2_attn_gemm1(const unsigned short* __restrict__ w1, const unsigned short* __restrict__ w3,
                                                        const unsigned short* __restrict__ w2, char* __restrict__ ws) {
  extern __shared__ __attribute__((aligned(16))) char smem[];
  const int lane = threadIdx.x & 63;
  const int w = __builtin_amdgcn_readfirstlane(threadIdx.x >> 6);
  const int u = blockIdx.x;
  STAMP_AT(1 + (MODE == 2) * 3, 0);
  if (MODE == 1) { attn_unit(smem, ws, u, w, lane); STAMP_AT(1, 1); return; }
  if (MODE == 2) { gemm1_unit(smem, ws, w1, w3, w2, u, w, lane); STAMP_AT(4, 1); return; }
  if (u < 256) {
#define GPRIO 1
    __builtin_amdgcn_s_setprio(GPRIO);
    gemm1_unit(smem, ws, w1, w3, w2, u, w, lane);
  } else {
    attn_unit(smem, ws, u - 256, w, lane);
  }
  STAMP_AT(1, 1);
}

#define PSTORE(p, v) store16_wt(p, v)
#define PSLEEP 1
constexpr int G2_STAGE = 18432, K3_LDS = 8 * G2_STAGE;
template <int OFF>
DI void bload16(u32x4& a, __amdgpu_buffer_rsrc_t r, unsigned voff, unsigned soff) {
  asm volatile("buffer_load_dwordx4 %0, %1, %2, %3 offen offset:%4" : "=v"(a) : "v"(voff), "s"(r), "s"(soff), "i"(OFF) : "memory");
}
template <int OFF>
DI void blds16(__amdgpu_buffer_rsrc_t r, unsigned voff, unsigned soff) {
  asm volatile("buffer_load_dwordx4 %0, %1, %2 offen offset:%3 lds" ::"v"(voff), "s"(r), "s"(soff), "i"(OFF) : "memory");
}
template <int OFF>
DI void blds4(__amdgpu_buffer_rsrc_t r, unsigned voff, unsigned soff) {
  asm volatile("buffer_load_dword %0, %1, %2 offen offset:%3 lds" ::"v"(voff), "s"(r), "s"(soff), "i"(OFF) : "memory");
}
template <int ADD>
DI void set_m0(unsigned base) { asm volatile("s_add_u32 m0, %0, %1" ::"s"(base), "i"(ADD) : "memory", "scc"); }
template <int N>
DI void wait_bar(u32x4& a, u32x4& b) { asm volatile("s_waitcnt vmcnt(%2)\n\ts_barrier" : "+v"(a), "+v"(b) : "i"(N) : "memory"); }

template <int T>
DI void g2_iter(f32x4 (&acc)[2][9], bf16x8 (&af)[9], u32x4 (&hf)[6][2], const lds_cptr (&ap)[3], __amdgpu_buffer_rsrc_t rw, __amdgpu_buffer_rsrc_t rh,
                unsigned w16, unsigned w4, unsigned hoff, unsigned hoff1, unsigned wdst) {
  constexpr int X = T + 1, NH = (T + 4 < 20 ? T + 4 : 20) - X + 1, NW = (T + 5 < 20 ? T + 5 : 20) - X;
  wait_bar<(T == 20 ? 0 : 2 * NH + 3 * NW)>(hf[T % 6][0], hf[T % 6][1]);
  SBAR();
#pragma unroll
  for (int b = 0; b < 9; ++b) {
    MFMA16A(acc[0][b], af[b], hf[T % 6][0]);
    SBAR();
    if (T + 5 <= 20 && b == 0) bload16<0>(hf[(T + 5) % 6][0], rh, hoff, (T + 5) * 4096);
    if (T + 5 <= 20 && b == 1) bload16<0>(hf[(T + 5) % 6][1], rh, hoff1, (T + 5) * 4096);
    if (T + 6 <= 20 && b == 2) set_m0<((T + 6) & 7) * G2_STAGE>(wdst);
    if (T + 6 <= 20 && b == 3) blds16<0>(rw, w16, (T + 6) * G2_STAGE);
    if (T + 6 <= 20 && b == 5) blds16<1024>(rw, w16, (T + 6) * G2_STAGE);
    if (T + 6 <= 20 && b == 7) blds4<2048>(rw, w4, (T + 6) * G2_STAGE);
    SBAR();
    MFMA16A(acc[1][b], af[b], hf[T % 6][1]);
    SBAR();
    if (T < 20) af[b] = LDS128(ap[((T + 1) & 7) / 3] + (((T + 1) & 7) % 3) * G2_STAGE + b * 1024);
    SBAR();
  }
}
template <int T, int E, typename... A>
DI void g2_run(A&... a) { if constexpr (T < E) { g2_iter<T>(a...); g2_run<T + 1, E>(a...); } }

__global__ __launch_bounds__(512, 2) void k3_gemm2(const unsigned short* __restrict__ x, char* __restrict__ ws, unsigned short* __restrict__ out) {
  extern __shared__ __attribute__((aligned(16))) char smem[];
  const unsigned lds0 = (unsigned)(uintptr_t)smem;
  const int lane = threadIdx.x & 63;
  const int w = __builtin_amdgcn_readfirstlane(threadIdx.x >> 6);
  const int xcd = blockIdx.x & 7, j = blockIdx.x >> 3;
  const int sp = xcd >> 1, rt = (xcd & 1) * 8 + (j >> 2), ct = j & 3;
  const int wm = w & 3, kg = w >> 2;
  const int c = lane & 15, q = lane >> 4;
  STAMP_DECL;
  STAMP_R(0);
  const __amdgpu_buffer_rsrc_t rw = __builtin_amdgcn_make_buffer_rsrc(ws + WS_WP + (size_t)(ct * 81 + sp * 20) * G2_STAGE + w * 2304, 0, 21 * G2_STAGE, 0x00020000);
  const __amdgpu_buffer_rsrc_t rh = __builtin_amdgcn_make_buffer_rsrc(ws + WS_H + (size_t)(rt * 4 + wm) * NBLK * 1024 + sp * 81920 + kg * 2048, 0, 21 * 4096, 0x00020000);
  unsigned w16 = lane * 16, w4 = lane * 4, wdst = lds0 + w * 2304;
  unsigned hoff = (q >> 1) * 1024 + (q & 1) * 512 + c * 16 + kg * 256, hoff1 = hoff ^ 256;
  u32x4 hf[6][2];
#define G2_PW(s) do { set_m0<(s) * G2_STAGE>(wdst); asm volatile("s_nop 0"); blds16<0>(rw, w16, (s) * G2_STAGE); blds16<1024>(rw, w16, (s) * G2_STAGE); blds4<2048>(rw, w4, (s) * G2_STAGE); } while (0)
#define G2_PH(s) do { bload16<0>(hf[s][0], rh, hoff, (s) * 4096); bload16<0>(hf[s][1], rh, hoff1, (s) * 4096); } while (0)
#define G2_PB() asm volatile("s_barrier" ::: "memory")
  G2_PW(0); G2_PH(0); G2_PB(); G2_PW(1); G2_PH(1); G2_PB(); G2_PW(2); G2_PH(2); G2_PB(); G2_PW(3); G2_PH(3); G2_PB(); G2_PW(4); G2_PH(4); G2_PB(); G2_PW(5);
#undef G2_PB
#undef G2_PW
#undef G2_PH
  f32x4 acc[2][9];
#pragma unroll
  for (int i = 0; i < 2; ++i)
#pragma unroll
    for (int b = 0; b < 9; ++b) { acc[i][b] = f32x4{0.f, 0.f, 0.f, 0.f}; PIN(acc[i][b]); }
  lds_cptr ap[3];
#pragma unroll
  for (int i = 0; i < 3; ++i) { ap[i] = (lds_cptr)smem + i * 3 * G2_STAGE + kg * 9216 + lane * 16; PIN(ap[i]); }
  bf16x8 af[9];
  asm volatile("s_waitcnt vmcnt(25)\n\ts_barrier" ::: "memory");
  STAMP_R(4);
#pragma unroll
  for (int b = 0; b < 9; ++b) af[b] = LDS128(ap[0] + b * 1024);
  g2_run<0, 6>(acc, af, hf, ap, rw, rh, w16, w4, hoff, hoff1, wdst);
  g2_run<6, 14>(acc, af, hf, ap, rw, rh, w16, w4, hoff, hoff1, wdst);
  g2_run<14, 20>(acc, af, hf, ap, rw, rh, w16, w4, hoff, hoff1, wdst);
  if (sp == 3) g2_iter<20>(acc, af, hf, ap, rw, rh, w16, w4, hoff, hoff1, wdst);
  STAMP_R(1);
  asm volatile("s_nop 7\n\ts_waitcnt vmcnt(0) lgkmcnt(0)\n\ts_barrier" : "+v"(hf[20 % 6][0]), "+v"(hf[20 % 6][1]) :: "memory");
#pragma unroll
  for (int b = 0; b < 9; ++b) { PIN(acc[0][b]); PIN(acc[1][b]); }
  f32x4* red = (f32x4*)(smem + (wm * 2) * 9216) + lane;
#pragma unroll
  for (int b = 0; b < 9; ++b) red[(kg ^ 1) * 576 + b * 64] = acc[1][b];
  BARRIER_LGKM();
  STAMP_R(7);
  const size_t orow = (size_t)(rt * 128 + wm * 32 + kg * 16) * D_ + ct * 144;
  const size_t oi = orow + (lane >> 2) * D_ + sp * 32 + (lane & 3) * 8, ot = orow + ((lane & 31) >> 1) * D_ + 128 + (lane & 1) * 8;
  const u32x4 xv = *(const u32x4*)(x + oi), xt = *(const u32x4*)(x + ot);
  f32x4 v[10];
#pragma unroll
  for (int b = 0; b < 9; ++b) v[b] = acc[0][b] + red[kg * 576 + b * 64];
#pragma unroll
  for (int i = 0; i < 4; ++i) v[9][i] = __uint_as_float(__builtin_amdgcn_permlane32_swap(__float_as_uint(v[8][i]), __float_as_uint(v[8][i]), false, false)[1]);
  char* box = ws + WS_P + (size_t)(rt * 4 + ct) * (12 * 9216) + lane * 16;
#define PK8(a, b) u32x4{min(cvtpk(a[0], a[1]), ~1u), min(cvtpk(a[2], a[3]), ~1u), min(cvtpk(b[0], b[1]), ~1u), min(cvtpk(b[2], b[3]), ~1u)}
#pragma unroll
  for (int j = 0; j < 4; ++j)
    if (j != sp) store16_wt(box + (j * 3 + ((sp - j - 1) & 3)) * 9216 + w * 1024, PK8(v[2 * j], v[2 * j + 1]));
  if (wm != sp && lane < 32) store16_wt(box + (wm * 3 + ((sp - wm - 1) & 3)) * 9216 + 8192 + kg * 512, PK8(v[8], v[9]));
#undef PK8
  f32x4 lo = v[0], hi = v[1];
#pragma unroll
  for (int j = 1; j < 4; ++j)
    if (j == sp) { lo = v[2 * j]; hi = v[2 * j + 1]; }
  const char* in = box + sp * 3 * 9216 + w * 1024;
  const char* tin = box + sp * 3 * 9216 + 8192 + kg * 512;
  u32x4 r[6];
  unsigned long long m[6];
#pragma unroll
  for (int i = 0; i < 6; ++i) { r[i] = u32x4{0u, 0u, 0u, 0u}; m[i] = i < 3 ? ~0ull : wm == sp ? 0xFFFFFFFFull : 0ull; }
  STAMP_R(5);
  for (int spin = 0; spin < (1 << 20); ++spin) {
    asm volatile("s_mov_b64 exec, %12\n\tglobal_load_dwordx4 %0, %6, off sc1\n\ts_mov_b64 exec, %13\n\tglobal_load_dwordx4 %1, %7, off sc1\n\t"
                 "s_mov_b64 exec, %14\n\tglobal_load_dwordx4 %2, %8, off sc1\n\ts_mov_b64 exec, %15\n\tglobal_load_dwordx4 %3, %9, off sc1\n\t"
                 "s_mov_b64 exec, %16\n\tglobal_load_dwordx4 %4, %10, off sc1\n\ts_mov_b64 exec, %17\n\tglobal_load_dwordx4 %5, %11, off sc1\n\t"
                 "s_mov_b64 exec, -1\n\ts_waitcnt vmcnt(0)"
                 : "+v"(r[0]), "+v"(r[1]), "+v"(r[2]), "+v"(r[3]), "+v"(r[4]), "+v"(r[5])
                 : "v"(in), "v"(in + 9216), "v"(in + 18432), "v"(tin), "v"(tin + 9216), "v"(tin + 18432),
                   "s"(m[0]), "s"(m[1]), "s"(m[2]), "s"(m[3]), "s"(m[4]), "s"(m[5]) : "memory");
#pragma unroll
    for (int i = 0; i < 6; ++i) m[i] = __ballot(max(max(r[i][0], r[i][1]), max(r[i][2], r[i][3])) == ~0u);
    if (spin == 0) STAMP_R(6);
    stc_[4] = stc_[0] + spin;
    if (!(m[0] | m[1] | m[2] | m[3] | m[4] | m[5])) break;
    __builtin_amdgcn_s_sleep(PSLEEP);
  }
  STAMP_R(3);
#define FIN(o, xx, a, b, r0, src) _Pragma("unroll") for (int k = 0; k < 4; ++k) { \
    const float e = (k < 2 ? a[2 * k] : b[2 * k - 4]) + bflo(r[r0][k]) + (bflo(r[r0 + 1][k]) + bflo(r[r0 + 2][k])); \
    const float f = (k < 2 ? a[2 * k + 1] : b[2 * k - 3]) + bfhi(r[r0][k]) + (bfhi(r[r0 + 1][k]) + bfhi(r[r0 + 2][k])); \
    o[k] = cvtpk(bflo(xx[k]) + __shfl(e, src), bfhi(xx[k]) + __shfl(f, src)); }
  u32x4 o;
  FIN(o, xv, lo, hi, 0, (lane & 3) * 16 + (lane >> 2));
  *(u32x4*)(out + oi) = o;
  if (wm == sp) {
    FIN(o, xt, v[8], v[9], 3, (lane & 1) * 16 + ((lane & 31) >> 1));
    if (lane < 32) *(u32x4*)(out + ot) = o;
  }
#undef FIN
  STAMP_R(2);
  STAMP_FLUSH(2, 0, 7);
}

__global__ void k_diag(char* ws) {
  __shared__ unsigned long long mn[3][256], mx[3][256];
  const unsigned long long* st = (const unsigned long long*)(ws + (200u << 20));
  const int t = threadIdx.x;
  unsigned long long a[3] = {~0ull, ~0ull, ~0ull}, b[3] = {0, 0, 0};
  for (int wv = 0; wv < 4; ++wv) { const unsigned long long* p = st + ((size_t)t * 8 + wv) * 16; a[0] = min(a[0], p[0]); b[0] = max(b[0], p[6]); }
  for (int k = 0; k < 2; ++k) for (int wv = 0; wv < 4; ++wv) { const unsigned long long* p = st + ((size_t)8192 + (t + 256 * k) * 8 + wv) * 16; a[1] = min(a[1], p[0]); b[1] = max(b[1], p[2]); }
  for (int wv = 0; wv < 8; ++wv) { const unsigned long long* p = st + ((size_t)2 * 8192 + t * 8 + wv) * 16; a[2] = min(a[2], p[0]); b[2] = max(b[2], p[DIAG == 6 ? 14 : DIAG == 7 ? 8 : 4]); }
  for (int i = 0; i < 3; ++i) { mn[i][t] = a[i]; mx[i][t] = b[i]; }
  __syncthreads();
  if (t == 0) {
    for (int j = 0; j < 256; ++j) for (int i = 0; i < 3; ++i) { a[i] = min(a[i], mn[i][j]); b[i] = max(b[i], mx[i][j]); }
    long long m = DIAG == 1 ? b[0] - a[0] : DIAG == 2 ? a[1] - b[0] : DIAG == 3 ? b[1] - a[1] : DIAG == 4 ? a[2] - b[1] : DIAG >= 5 ? b[2] - a[2] : 0;
    m = m < 0 ? 0 : m > 10000 ? 10000 : m;
    const unsigned long long t0 = __builtin_amdgcn_s_memrealtime();
    while (__builtin_amdgcn_s_memrealtime() < t0 + 4 * m) __builtin_amdgcn_s_sleep(1);
  }
}
extern "C" void kernel_launch(void* const* d_in, const int* in_sizes, int n_in, void* d_out, int out_size, void* d_ws, size_t ws_size,
                              hipStream_t stream) {
  const unsigned short* x = (const unsigned short*)d_in[0];
  const unsigned short* wq = (const unsigned short*)d_in[1];
  const unsigned short* wk = (const unsigned short*)d_in[2];
  const unsigned short* wv = (const unsigned short*)d_in[3];
  const unsigned short* wo = (const unsigned short*)d_in[4];
  const unsigned short* w1 = (const unsigned short*)d_in[6];
  const unsigned short* w3 = (const unsigned short*)d_in[7];
  const unsigned short* w2 = (const unsigned short*)d_in[8];
  char* ws = (char*)d_ws;
  (void)hipFuncSetAttribute((const void*)k1_ln_qkv, hipFuncAttributeMaxDynamicSharedMemorySize, K1_LDS);
  (void)hipFuncSetAttribute((const void*)k3_gemm2, hipFuncAttributeMaxDynamicSharedMemorySize, K3_LDS);
  k1_ln_qkv<<<256, 512, K1_LDS, stream>>>(x, wq, wk, wv, wo, w2, ws);
  (void)hipFuncSetAttribute((const void*)k2_attn_gemm1<0>, hipFuncAttributeMaxDynamicSharedMemorySize, K2_LDS);
  k2_attn_gemm1<0><<<512, 256, K2_LDS, stream>>>(w1, w3, w2, ws);
  k3_gemm2<<<256, 512, K3_LDS, stream>>>(x, ws, (unsigned short*)d_out);
  k_diag<<<1, 256, 0, stream>>>(ws);
}
```

```cpp
#include <hip/hip_runtime.h>
#include <hip/hip_bf16.h>
#include <stdint.h>

typedef short bf16x8 __attribute__((ext_vector_type(8)));
typedef short bf16x4 __attribute__((ext_vector_type(4)));
typedef float f32x4 __attribute__((ext_vector_type(4)));
typedef float f32x16 __attribute__((ext_vector_type(16)));
typedef unsigned u32x4 __attribute__((ext_vector_type(4)));
typedef unsigned u32x2 __attribute__((ext_vector_type(2)));
typedef unsigned u32x4_a4 __attribute__((ext_vector_type(4), aligned(4)));
typedef __attribute__((address_space(3))) const char* lds_cptr;
typedef __attribute__((address_space(3))) char* lds_ptr;
#define DI __device__ __forceinline__

constexpr int S_ = 2048, D_ = 576, F_ = 4608;
constexpr int NBLK = 324;
constexpr int KV_HS = 20480;
constexpr int KV_K2 = 8192, KV_V = 10240;
constexpr size_t WS_XN = 0;
constexpr size_t WS_Q = WS_XN + (size_t)S_ * D_ * 2;
constexpr size_t WS_KV = WS_Q + (size_t)S_ * D_ * 2;
constexpr size_t WS_KMAX = WS_KV + 8 * KV_HS;
constexpr size_t WS_WP = WS_KMAX + 1024;
constexpr size_t WS_H = WS_WP + (size_t)4 * 162 * 9216;
constexpr size_t WS_P = WS_H + (size_t)64 * NBLK * 1024;
constexpr size_t WS_IN1 = WS_P + (size_t)256 * 28672;
constexpr size_t WS_PKF = WS_IN1 + 256 * 1024;
constexpr size_t WS_KVF = WS_PKF + 2048;
constexpr size_t WS_E = WS_KVF + 32 * 256;
constexpr int IN_Q = 64, IN_H = 80, IN_O = 96, IN_B = 112;
constexpr int SY = 163072, SY_XN = SY, SY_MF = SY + 4, SY_Q = SY + 8, SY_PKS = SY + 16, SY_A = SY + 20, SY_G = SY + 24, SY_BAD = SY + 28,
              SY_AE = SY + 32, SY_GE = SY + 36, LDS_ALL = SY + 64;

DI unsigned cvtpk(float lo, float hi) {
  typedef __bf16 b2 __attribute__((ext_vector_type(2)));
  typedef float f2 __attribute__((ext_vector_type(2)));
  f2 v = {lo, hi};
  return __builtin_bit_cast(unsigned, __builtin_convertvector(v, b2));
}
DI float bflo(unsigned u) { return __uint_as_float(u << 16); }
DI float bfhi(unsigned u) { return __uint_as_float(u & 0xffff0000u); }
DI void glds16(const void* sbase, unsigned voff, unsigned lds_addr) {
  asm volatile("s_mov_b32 m0, %0\n\ts_nop 0\n\tglobal_load_lds_dwordx4 %1, %2" ::"s"(lds_addr), "v"(voff), "s"(sbase) : "memory");
}
DI void store16_wt(void* p, u32x4 v) {
  asm volatile("global_store_dwordx4 %0, %1, off sc1\n\ts_nop 1" ::"v"(p), "v"(v) : "memory");
}

typedef unsigned long long u64;
#define GSPIN (1 << 13)
#define LSPIN (1 << 24)
DI void store8_wt(void* p, u32x2 v) { asm volatile("global_store_dwordx2 %0, %1, off sc1\n\ts_nop 1" ::"v"(p), "v"(v) : "memory"); }
DI void store4_wt(void* p, unsigned v) { asm volatile("global_store_dword %0, %1, off sc1\n\ts_nop 1" ::"v"(p), "v"(v) : "memory"); }
#define DRAIN() asm volatile("s_waitcnt vmcnt(0)" ::: "memory")
DI u64 poll_words(const char* p, u64 need, u32x2 tag) {
  u64 ok = 0;
#pragma nounroll
  for (int spin = 0; spin < GSPIN; ++spin) {
    u32x2 v;
    asm volatile("global_load_dwordx2 %0, %1, off sc1\n\ts_waitcnt vmcnt(0)" : "=v"(v) : "v"(p) : "memory");
    ok = __ballot(v[0] == tag[0] && v[1] == tag[1]);
    if ((ok & need) == need) break;
    __builtin_amdgcn_s_sleep(2);
  }
  return ok;
}
typedef volatile __attribute__((address_space(3))) int* lds_word;
DI void lds_arrive(unsigned addr) {
  asm volatile("s_mov_b64 exec, 1\n\tds_add_u32 %0, %1\n\ts_mov_b64 exec, -1" ::"v"(addr), "v"(1u) : "memory");
}
DI int lds_arrive_rtn(unsigned addr) {
  int r;
  asm volatile("s_mov_b64 exec, 1\n\tds_add_rtn_u32 %0, %1, %2\n\ts_mov_b64 exec, -1\n\ts_waitcnt lgkmcnt(0)" : "=&v"(r) : "v"(addr), "v"(1u) : "memory");
  return __builtin_amdgcn_readfirstlane(r);
}
DI int lds_peek(unsigned addr) { return *(lds_word)(uintptr_t)addr; }
DI void lds_wait(unsigned addr, int target) {
#pragma nounroll
  for (int spin = 0; spin < LSPIN; ++spin) {
    if (__builtin_amdgcn_readfirstlane(lds_peek(addr)) >= target) break;
    __builtin_amdgcn_s_sleep(1);
  }
  asm volatile("" ::: "memory");
}
#define LDS_CHECK(v, addr, target) do { if (__builtin_expect(__builtin_amdgcn_readfirstlane(v) < (target), 0)) lds_wait(addr, target); asm volatile("" ::: "memory"); } while (0)

DI bf16x4 trread(lds_cptr p) {
  return __builtin_amdgcn_ds_read_tr16_b64_v4i16((__attribute__((address_space(3))) bf16x4*)p);
}
DI bf16x8 cat(bf16x4 a, bf16x4 b) { return __builtin_shufflevector(a, b, 0, 1, 2, 3, 4, 5, 6, 7); }
#define MFMA32(a, b, c) __builtin_amdgcn_mfma_f32_32x32x16_bf16((a), (b), (c), 0, 0, 0)
#define MFMA32K8(a, b, c) __builtin_amdgcn_mfma_f32_32x32x8bf16_1k((a), (b), (c), 0, 0, 0)
#define MFMA16(a, b, c) __builtin_amdgcn_mfma_f32_16x16x32_bf16((a), (b), (c), 0, 0, 0)
#define MFMA16A(c, a, b) asm volatile("v_mfma_f32_16x16x32_bf16 %0, %1, %2, %0" : "+v"(c) : "v"(a), "v"(b))
#define MFMA_DRAIN() asm volatile("s_nop 7\n\ts_nop 7" ::: "memory")
#define SBAR() __builtin_amdgcn_sched_barrier(0)
#define PIN(x) asm volatile("" : "+v"(x))
#define LDS128(p) (*(const __attribute__((address_space(3))) bf16x8*)(p))
#define LDS64(p) (*(const __attribute__((address_space(3))) bf16x4*)(p))
#define WAIT_VM0() __builtin_amdgcn_s_waitcnt(0x0F70)
#define BARRIER_LGKM() asm volatile("s_waitcnt lgkmcnt(0)\n\ts_barrier" ::: "memory")
#define STAMP_AT(kid, slot)
#define STAMP_DECL
#define STAMP_R(slot)
#define STAMP_FLUSH(kid, lo, hi)

constexpr int K1_XT = 144 * 800, PK_BUF = 3 * KV_HS;
typedef float f32x2 __attribute__((ext_vector_type(2)));
typedef __attribute__((address_space(3))) u32x4* lds_v4;
DI float quad_sum(float v) {
  const auto a = __builtin_amdgcn_permlane32_swap(__float_as_uint(v), __float_as_uint(v), false, false);
  const float s = __uint_as_float(a[0]) + __uint_as_float(a[1]);
  const auto b = __builtin_amdgcn_permlane16_swap(__float_as_uint(s), __float_as_uint(s), false, false);
  return __uint_as_float(b[0]) + __uint_as_float(b[1]);
}
DI u32x2 make_tag(u32x2 e) {
  const u64 t = (((u64)(unsigned)__builtin_amdgcn_readfirstlane(e[1]) << 32) | (unsigned)__builtin_amdgcn_readfirstlane(e[0])) + 1;
  return u32x2{(unsigned)t, (unsigned)(t >> 32)};
}

DI void p1_load(char* smem, const unsigned short* wq, const unsigned short* wk, const unsigned short* wv, int g, int tg, int w, int lane) {
  const unsigned lds0 = (unsigned)(uintptr_t)smem;
  asm volatile("s_waitcnt lgkmcnt(0)\n\ts_barrier" ::: "memory");
  if (g == 7) {
    for (int k = (w - 4) * 64 + lane; k < 576; k += 256) {
      const u32x4* a = (const u32x4*)(wq + (size_t)k * D_ + 560);
      const u32x4 a0 = a[0], a1 = a[1];
      const unsigned* kp = (const unsigned*)(wk + k * 18);
      const unsigned* vp = (const unsigned*)(wv + k * 18);
      const u32x4 k0 = *(const u32x4_a4*)kp, k1 = *(const u32x4_a4*)(kp + 4);
      const unsigned k2 = kp[8];
      const u32x4 v0 = *(const u32x4_a4*)vp, v1 = *(const u32x4_a4*)(vp + 4);
      const unsigned v2 = vp[8];
      char* d = smem + ((k >> 2) ^ ((k >> 3) & 1)) * 800 + (k & 3) * 192;
      *(u32x4*)(d) = a0; *(u32x4*)(d + 16) = a1;
      *(u32x4*)(d + 32) = k0; *(u32x4*)(d + 48) = k1; *(unsigned*)(d + 64) = k2;
      *(u32x4*)(d + 96) = v0; *(u32x4*)(d + 112) = v1; *(unsigned*)(d + 128) = v2;
    }
  } else if (lane < 48) {
    const unsigned row = lane / 12u, voff = row * 1152u + (lane - row * 12u) * 16u;
    const char* base = (const char*)wq + ((160 * g) & ~63) + ((w & 3) ^ ((w >> 1) & 1)) * 4608;
    int u = (tg & 3) * 9;
#pragma unroll 4
    for (int i = 0; i < 36; ++i) {
      glds16(base + u * 18432, voff, lds0 + (w & 3) * 800 + u * 3200);
      u = u == 35 ? 0 : u + 1;
    }
  }
}
DI void pack_load(char* smem, const unsigned short* wo, const unsigned short* w2, int job, int w, int lane) {
  const int j = job >> 2;
  const unsigned ra = j < 144 ? 16 : j < 160 ? 18 : 144, rb = j < 160 ? 2 : 18, rc = j < 144 ? 32 * j : j < 160 ? 36 * (j - 144) : 288 * (j - 160) + 16;
  const char* base = (const char*)(j < 144 ? w2 : wo) + (size_t)rc * 1152 + (job & 3) * 288;
#pragma unroll
  for (int v = 0; v < 9; ++v) {
    const unsigned ch = v * 64 + lane, row = ch / 18u, cc = ch - row * 18u;
    const unsigned src = ra * (row >> 4) + rb * ((row & 15) >> 1) + (row & 1);
    glds16(base, src * 1152u + cc * 16u, (unsigned)(uintptr_t)smem + PK_BUF + w * 9216 + v * 1024);
  }
}
DI void pack_store(char* smem, char* ws, int job, int w, int lane) {
  const int c = lane & 15, q = lane >> 4;
  const lds_cptr pk = (lds_cptr)smem + PK_BUF + w * 9216 + (16 * (q >> 1) + 4 * (q & 1) + (c >> 2)) * 288;
  char* wp = ws + WS_WP + (size_t)((job & 3) * 162 + (job >> 2)) * 9216 + lane * 16;
#pragma unroll
  for (int b = 0; b < 9; ++b) {
    const lds_cptr a = pk + (b < 8 ? 64 * (b >> 1) + 8 * (b & 1) + 16 * (c & 3) : 256 + 16 * (c & 1) + 8 * ((c >> 1) & 1));
    store16_wt(wp + b * 1024, __builtin_bit_cast(u32x4, cat(trread(a), trread(a + 8 * 288))));
  }
}

DI u32x2 p1_compute(char* smem, const unsigned short* x, char* ws, int g, int tg, int w, int lane, u32x2 e) {
  const unsigned lds0 = (unsigned)(uintptr_t)smem;
  const int c = lane & 15, q = lane >> 4;
  STAMP_DECL;
  STAMP_R(0);
  const int tok = tg * 64 + w * 16 + c;
  u32x4 xr[18];
  {
    const char* xp = (const char*)x + (tg * 64 + w * 16 + (c & 12)) * 1152 + q * 64 + (c & 3) * 16;
#pragma unroll
    for (int i = 0; i < 16; ++i) xr[i] = *(const u32x4*)(xp + (i & 3) * 1152 + (i >> 2) * 256);
#pragma unroll
    for (int i = 16; i < 18; ++i) xr[i] = *(const u32x4*)((const char*)x + tok * 1152 + i * 64 + q * 16);
    asm volatile("s_barrier" ::: "memory");
    STAMP_R(6);
    const lds_ptr tw = (lds_ptr)smem + K1_XT + w * 4352 + (c & 12) * 272 + q * 64 + (c & 3) * 16, tr = (lds_ptr)smem + K1_XT + w * 4352 + c * 272 + q * 16;
#pragma unroll
    for (int gq = 0; gq < 4; ++gq) {
#pragma unroll
      for (int i = 0; i < 4; ++i) *(lds_v4)(tw + i * 272) = xr[4 * gq + i];
      asm volatile("" ::: "memory");
#pragma unroll
      for (int i = 0; i < 4; ++i) xr[4 * gq + i] = *(lds_v4)(tr + i * 64);
      asm volatile("" ::: "memory");
    }
    STAMP_R(4);
  }
  f32x2 d[72];
  f32x2 s2[2] = {{0.f, 0.f}, {0.f, 0.f}};
#pragma unroll
  for (int i = 0; i < 72; ++i) { d[i] = f32x2{bflo(xr[i >> 2][i & 3]), bfhi(xr[i >> 2][i & 3])}; s2[i & 1] += d[i]; }
  const float mean = quad_sum((s2[0][0] + s2[0][1]) + (s2[1][0] + s2[1][1])) * (1.f / 576.f);
  f32x2 v2[2] = {{0.f, 0.f}, {0.f, 0.f}};
#pragma unroll
  for (int i = 0; i < 72; ++i) { d[i] -= mean; v2[i & 1] += d[i] * d[i]; }
  const float rstd = 1.f / sqrtf(quad_sum((v2[0][0] + v2[0][1]) + (v2[1][0] + v2[1][1])) * (1.f / 576.f) + 1e-5f);
#pragma unroll
  for (int i = 0; i < 72; ++i) { const f32x2 y = d[i] * rstd; xr[i >> 2][i & 3] = cvtpk(y[0], y[1]); }
  {
    char* xn = ws + WS_XN + (size_t)(tok >> 5) * 36864 + (w & 1) * 1024 + lane * 16;
#pragma unroll
    for (int i = 0; i < 18; ++i)
      if ((i & 7) == g) store16_wt(xn + i * 2048, xr[i]);
  }
  STAMP_R(1);
  asm volatile("s_barrier" ::: "memory");
  STAMP_R(2);
  f32x4 acc[5];
#pragma unroll
  for (int b = 0; b < 5; ++b) acc[b] = f32x4{0.f, 0.f, 0.f, 0.f};
  const int co = (c >> 2) * 192 + 8 * (c & 3) + (g < 7 ? (g & 1) * 32 : 0);
  const lds_cptr p0 = (lds_cptr)smem + (2 * q + (q & 1)) * 800 + co, p1 = (lds_cptr)smem + (2 * q + 1 - (q & 1)) * 800 + co;
  const lds_cptr p2 = p0 + 57600, p3 = p1 + 57600;
  bf16x4 al[10], ah[10];
#define XNAT 40
#define K1_RD(n) do { al[(n) % 10] = trread(((n) < 45 ? p0 : p2) + ((n) % 45 / 5) * 6400 + ((n) % 5) * 32); \
                      ah[(n) % 10] = trread(((n) < 45 ? p1 : p3) + ((n) % 45 / 5) * 6400 + ((n) % 5) * 32); } while (0)
#define K1_XN(n) if ((n) == XNAT) { asm volatile("s_waitcnt vmcnt(0)" : "+v"(e) :: "memory"); lds_arrive(lds0 + SY_XN); }
#pragma unroll
  for (int n = 0; n < 9; ++n) K1_RD(n);
  if (g < 7) {
#pragma unroll
    for (int n = 0; n < 90; ++n) {
      if (n + 9 < 90) K1_RD(n + 9);
      acc[n % 5] = MFMA16(cat(al[n % 10], ah[n % 10]), __builtin_bit_cast(bf16x8, xr[n / 5]), acc[n % 5]);
      K1_XN(n);
      SBAR();
    }
  } else {
#pragma unroll
    for (int n = 0; n < 90; ++n) {
      if (n + 9 < 90) K1_RD(n + 9);
      if (n % 5 < 3) acc[n % 5] = MFMA16(cat(al[n % 10], ah[n % 10]), __builtin_bit_cast(bf16x8, xr[n / 5]), acc[n % 5]);
      else acc[n % 5] = MFMA16(__builtin_bit_cast(bf16x8, xr[n / 5]), cat(al[n % 10], ah[n % 10]), acc[n % 5]);
      K1_XN(n);
      SBAR();
    }
  }
#undef K1_RD
#undef K1_XN
  lds_arrive(lds0 + SY_MF);
  STAMP_R(7);
  constexpr float QSCALE = 0.23570226039551584f * 1.4426950408889634f;
  char* qp = ws + WS_Q + ((size_t)tok * D_ + 80 * g) * 2;
#define QPK(b) u32x2{cvtpk(acc[b][0] * QSCALE, acc[b][1] * QSCALE), cvtpk(acc[b][2] * QSCALE, acc[b][3] * QSCALE)}
  if (g < 7) {
#pragma unroll
    for (int b = 0; b < 4; b += 2) {
      const u32x2 ua = QPK(b), ub = QPK(b + 1);
      const auto s0 = __builtin_amdgcn_permlane16_swap(ua[0], ub[0], false, false), s1 = __builtin_amdgcn_permlane16_swap(ua[1], ub[1], false, false);
      store16_wt(qp + b * 32 + (q & 1) * 32 + (q >> 1) * 16, u32x4{s0[0], s1[0], s0[1], s1[1]});
    }
    store8_wt(qp + 128 + q * 8, QPK(4));
  } else {
    store8_wt(qp + q * 8, QPK(0));
    char* kv = ws + WS_KV + (tok >> 8) * KV_HS;
    const int kk = tok & 255;
    const u32x2 kA = {cvtpk(acc[1][0], acc[1][1]), cvtpk(acc[1][2], acc[1][3])};
    const unsigned kB = cvtpk(acc[2][0], acc[2][1]);
    store8_wt(kv + (kk >> 5) * 1024 + ((q >> 1) * 32 + (kk & 31)) * 16 + (q & 1) * 8, kA);
    if (q == 0) store8_wt(kv + KV_K2 + kk * 8, u32x2{kB, 0x3F803F80u});
    const int t0 = (tg * 64 + w * 16) & 255;
    char* vp = kv + KV_V + (t0 >> 4) * 624 + (q & 1) * 304 + (q >> 1) * 8;
    store8_wt(vp + c * 16, u32x2{cvtpk(acc[3][0], acc[3][1]), cvtpk(acc[3][2], acc[3][3])});
    if (c < 3 || (c == 3 && (q & 1)))
      store8_wt(vp + (16 + c) * 16, c < 2 ? u32x2{cvtpk(acc[4][0], acc[4][1]), cvtpk(acc[4][2], acc[4][3])} : c == 2 ? u32x2{0x3F803F80u, 0x3F803F80u} : u32x2{0u, 0u});
    float n2 = bflo(kA[0]) * bflo(kA[0]) + bfhi(kA[0]) * bfhi(kA[0]) + bflo(kA[1]) * bflo(kA[1]) + bfhi(kA[1]) * bfhi(kA[1]);
    if (q == 0) n2 += bflo(kB) * bflo(kB) + bfhi(kB) * bfhi(kB);
    n2 = quad_sum(n2);
    n2 = fmaxf(n2, __shfl_xor(n2, 1)); n2 = fmaxf(n2, __shfl_xor(n2, 2));
    n2 = fmaxf(n2, __shfl_xor(n2, 4)); n2 = fmaxf(n2, __shfl_xor(n2, 8));
    if (lane == 0) store4_wt(ws + WS_KMAX + (tg * 4 + w) * 4, __float_as_uint(n2));
  }
#undef QPK
  const u32x2 tag = make_tag(e);
  DRAIN();
  if (lds_arrive_rtn(lds0 + SY_Q) == 3) {
    if (lane < 8 || (g == 7 && lane >= 32))
      store8_wt(ws + (lane < 8 ? WS_IN1 + (tg * 8 + lane) * 1024 + (IN_Q + g) * 8 : WS_KVF + (lane - 32) * 256 + tg * 8), tag);
  }
  STAMP_R(3);
  STAMP_FLUSH(0, 0, 7);
  return tag;
}

#define KV_LOADS "global_load_dwordx4 %0, %5, %6 offset:-2048 sc1\n\tglobal_load_dwordx4 %1, %5, %6 offset:-1024 sc1\n\tglobal_load_dwordx4 %2, %5, %6 sc1\n\t" \
                 "global_load_dwordx4 %3, %5, %6 offset:1024 sc1\n\tglobal_load_dwordx4 %4, %5, %6 offset:2048 sc1\n"
DI void kv_load(u32x4 (&R)[5], const char* src, unsigned va) {
  asm volatile(KV_LOADS : "=&v"(R[0]), "=&v"(R[1]), "=&v"(R[2]), "=&v"(R[3]), "=&v"(R[4]) : "v"(va), "s"(src) : "memory");
}
DI void kv_turn(u32x4 (&R)[5], int hs, const char* src, unsigned va, const char* pkf, char* box, unsigned l16, unsigned l32, u32x4 ones) {
  asm volatile("s_cmp_lt_u32 %7, 6\n\ts_cbranch_scc0 1f\n\t" KV_LOADS "\ts_branch 2f\n"
               "1:\n\ts_cmp_lg_u32 %7, 6\n\ts_cbranch_scc1 2f\n\t"
               "global_load_dwordx4 %0, %11, %8 sc1\n\tglobal_load_dwordx4 %1, %11, %8 offset:16 sc1\n\t"
               "global_store_dwordx4 %10, %12, %9 offset:-3072 sc1\n\tglobal_store_dwordx4 %10, %12, %9 offset:-2048 sc1\n\t"
               "global_store_dwordx4 %10, %12, %9 offset:-1024 sc1\n\tglobal_store_dwordx4 %10, %12, %9 sc1\n\t"
               "global_store_dwordx4 %10, %12, %9 offset:1024 sc1\n\tglobal_store_dwordx4 %10, %12, %9 offset:2048 sc1\n\t"
               "global_store_dwordx4 %10, %12, %9 offset:3072 sc1\n"
               "2:"
               : "+v"(R[0]), "+v"(R[1]), "+v"(R[2]), "+v"(R[3]), "+v"(R[4])
               : "v"(va), "s"(src), "s"(hs), "s"(pkf), "s"(box), "v"(l16), "v"(l32), "v"(ones) : "memory", "scc");
}
#undef KV_LOADS
#define KV_WAIT(R) asm volatile("s_waitcnt vmcnt(0)" : "+v"(R[0]), "+v"(R[1]), "+v"(R[2]), "+v"(R[3]), "+v"(R[4]) :: "memory")
DI void kv_put(lds_ptr d, const u32x4 (&R)[5]) {
#pragma unroll
  for (int i = 0; i < 5; ++i) *(lds_v4)(d + i * 1024) = R[i];
}
DI void role_barrier(unsigned addr, int& tgt) {
  asm volatile("s_waitcnt lgkmcnt(0)" ::: "memory");
  lds_arrive(addr);
  tgt += 4;
  lds_wait(addr, tgt);
}
DI bf16x4 q_tail(unsigned q2, float m, int h) {
  const float m1 = bflo(cvtpk(m, 0.f)), r1 = m - m1, m2 = bflo(cvtpk(r1, 0.f)), m3 = r1 - m2;
  return __builtin_bit_cast(bf16x4, h ? u32x2{0u, cvtpk(-m3, 0.f)} : u32x2{q2, cvtpk(-m1, -m2)});
}

#define APRIO_HI 2
#define APRIO_LO 0
DI void attn_main(char* ws, char* smem, const unsigned short* wo, const unsigned short* w2, int job, int u, char* box, u32x2 tag, int& tgt, u32x4 (&R)[5], int w, int lane, bf16x8 qa1, bf16x4 qa2, bf16x8 qb1, bf16x4 qb2,
                  f32x16& oa, f32x16& ob) {
  const unsigned sya = (unsigned)(uintptr_t)smem + SY_A;
  const char* kvg = ws + WS_KV;
  const int r = lane & 31, h = lane >> 5;
  const unsigned va = w * 5120 + lane * 16;
  const int ok1 = lane * 16, ok2 = KV_K2 + r * 8, ov = KV_V + (r < 19 ? h * 304 + r * 16 : 608);
  const f32x16 zero = {};
  u32x4 ones = {~0u, ~0u, ~0u, ~0u};
  PIN(ones);
  STAMP_DECL;
  STAMP_R(2);
  role_barrier(sya, tgt);
  STAMP_R(3);
  bf16x8 k1 = LDS128((lds_cptr)smem + ok1), v0 = LDS128((lds_cptr)smem + ov), v1 = {};
  bf16x4 k2 = LDS64((lds_cptr)smem + ok2);
  f32x16 sa = MFMA32(k1, qa1, MFMA32K8(k2, qa2, zero)), sb;
  u32x4 p0, p1 = {0u, 0u, 0u, 0u};
  float e2 = 0.f, e3 = 0.f;
#define SLOT(MF, RD, NEXT, S, i, PP, jp, P, j) do { \
    MF; SBAR(); __builtin_amdgcn_s_setprio(APRIO_LO); SBAR(); \
    float e0 = __builtin_amdgcn_exp2f(S[i]); PIN(e0); SBAR(); RD; SBAR(); \
    float e1 = __builtin_amdgcn_exp2f(S[i + 1]); PIN(e1); SBAR(); \
    PP[jp] = cvtpk(e2, e3); PIN(PP); SBAR(); \
    e2 = __builtin_amdgcn_exp2f(S[i + 2]); PIN(e2); SBAR(); PIN(NEXT); SBAR(); \
    e3 = __builtin_amdgcn_exp2f(S[i + 3]); PIN(e3); SBAR(); __builtin_amdgcn_s_setprio(APRIO_HI); SBAR(); \
    P[j] = cvtpk(e0, e1); PIN(P); SBAR(); } while (0)
#define PB(x) __builtin_bit_cast(bf16x8, x)
  int c0 = 0, c1 = KV_HS, c2 = 2 * KV_HS, seen = 0;
  for (int hs = 0; hs < 8; ++hs) {
    const lds_cptr cur = (lds_cptr)smem + c0, nxt = (lds_cptr)smem + c1;
    const lds_cptr ck1 = cur + ok1, ck2 = cur + ok2, cv = cur + ov, nk1 = nxt + ok1, nk2 = nxt + ok2, nv = nxt + ov;
#pragma unroll
    for (int t = 0; t < 8; ++t) {
      if (t == 1) {
        KV_WAIT(R);
        kv_put((lds_ptr)smem + c1 + va, R);
        if (hs == 2 && job >= 0) pack_store(smem, ws, job, w, lane);
        if (hs == 3 && lds_arrive_rtn(sya - SY_A + SY_PKS) == 3 && lane == 0) store8_wt(ws + WS_PKF + u * 8, tag);
        if (hs == 7 && w == 0) {
          bool ok = true;
#pragma unroll
          for (int i = 0; i < 8; ++i) ok &= R[i >> 2][i & 3] == tag[i & 1];
          if (__builtin_expect(!__all(ok), 0))
#pragma nounroll
            for (int i = 0; i < 4; ++i) poll_words(ws + WS_PKF + i * 512 + lane * 8, ~0ull, tag);
          asm volatile("buffer_inv sc1" ::: "memory");
        }
      }
      if (t == 2) {
        lds_arrive(sya);
        tgt += 4;
        kv_turn(R, hs, kvg + ((hs + 2) & 7) * KV_HS, va + 2048, ws + WS_PKF, box, ok1, lane * 32, ones);
      }
      if (t == 3 && hs == 1 && job >= 0) pack_load(smem, wo, w2, job, w, lane);
      if (t == 6) seen = lds_peek(sya);
      if (t == 7) LDS_CHECK(seen, sya, tgt);
      SBAR();
      SLOT(sb = MFMA32K8(k2, qb2, zero), k2 = t < 7 ? LDS64(ck2 + (t + 1) * 256) : LDS64(nk2), e0, sa, 0, p1, 3, p0, 0);
      SLOT(ob = MFMA32(v1, PB(p1), ob), v1 = LDS128(cv + (2 * t + 1) * 624), e0, sa, 4, p0, 1, p0, 2);
      SLOT(sb = MFMA32(k1, qb1, sb), k1 = t < 7 ? LDS128(ck1 + (t + 1) * 1024) : LDS128(nk1), v0, sa, 8, p0, 3, p1, 0);
      SLOT(oa = MFMA32(v0, PB(p0), oa), , k2, sa, 12, p1, 1, p1, 2);
      SLOT(sa = MFMA32K8(k2, qa2, zero), , v1, sb, 0, p1, 3, p0, 0);
      SLOT(oa = MFMA32(v1, PB(p1), oa), , k1, sb, 4, p0, 1, p0, 2);
      SLOT(sa = MFMA32(k1, qa1, sa), , e0, sb, 8, p0, 3, p1, 0);
      SLOT(ob = MFMA32(v0, PB(p0), ob), v0 = t < 7 ? LDS128(cv + (2 * t + 2) * 624) : LDS128(nv), e0, sb, 12, p1, 1, p1, 2);
    }
    const int cc = c0; c0 = c1; c1 = c2; c2 = cc;
  }
#undef SLOT
  STAMP_R(4);
  STAMP_FLUSH(1, 2, 4);
  p1[3] = cvtpk(e2, e3);
  ob = MFMA32(v1, PB(p1), ob);
#undef PB
}

DI void attn_max(char* smem, const char* kvg, int& tgt, int w, int lane, bf16x8 qa1, bf16x4 qa2, bf16x8 qb1, bf16x4 qb2, float& ma, float& mb) {
  const unsigned sya = (unsigned)(uintptr_t)smem + SY_A;
  const unsigned va = w * 5120 + lane * 16;
  const f32x16 zero = {};
  for (int hs = 0; hs < 8; ++hs) {
    u32x4 R[5];
    kv_load(R, kvg + hs * KV_HS, va + 2048);
    KV_WAIT(R);
    kv_put((lds_ptr)smem + va, R);
    role_barrier(sya, tgt);
#pragma unroll 1
    for (int t = 0; t < 8; ++t) {
      const bf16x8 k1 = LDS128((lds_cptr)smem + lane * 16 + t * 1024);
      const bf16x4 k2 = LDS64((lds_cptr)smem + KV_K2 + (lane & 31) * 8 + t * 256);
      const f32x16 sa = MFMA32(k1, qa1, MFMA32K8(k2, qa2, zero)), sb = MFMA32(k1, qb1, MFMA32K8(k2, qb2, zero));
#pragma unroll
      for (int i = 0; i < 16; ++i) { ma = fmaxf(ma, sa[i]); mb = fmaxf(mb, sb[i]); }
    }
    role_barrier(sya, tgt);
  }
  ma = fmaxf(ma, __shfl_xor(ma, 32));
  mb = fmaxf(mb, __shfl_xor(mb, 32));
}

DI unsigned attn_store(char* hp, int hd, int lane, const f32x16& o, float l) {
  const float inv = 1.f / l;
  store16_wt(hp + (288 + hd) * 1024 + lane * 16,
             (u32x4{cvtpk(o[0] * inv, o[1] * inv), cvtpk(o[2] * inv, o[3] * inv), cvtpk(o[4] * inv, o[5] * inv), cvtpk(o[6] * inv, o[7] * inv)}));
  return cvtpk(o[8] * inv, o[9] * inv);
}

DI float half_sum(float v) {
  const auto t = __builtin_amdgcn_permlane32_swap(__float_as_uint(v), __float_as_uint(v), false, false);
  return __uint_as_float(t[0]) + __uint_as_float(t[1]);
}
constexpr int G2_STAGE = 18432;
DI void attn_unit(char* smem, char* ws, const unsigned short* wo, const unsigned short* w2, u32x2 tag, int u, int w, int lane) {
  const unsigned lds0 = (unsigned)(uintptr_t)smem;
  const char* kvg = ws + WS_KV;
  const int gw = u * 4 + w, hd = (gw & 15) * 2, tb = gw >> 4;
  const int r = lane & 31, h = lane >> 5;
  const unsigned va = w * 5120 + lane * 16;
  STAMP_AT(1, 0);
  poll_words(ws + (lane < 32 ? WS_KVF + (u & 31) * 256 + lane * 8 : WS_IN1 + u * 1024 + (IN_Q + (lane & 7)) * 8), 0xFFFFFFFFull | (1ull << (32 + 18 * hd / 80)) | (1ull << (32 + (18 * hd + 35) / 80)), tag);
  STAMP_AT(1, 5);
  u32x4 P[5], R[5], qa, qb;
  unsigned qa2, qb2;
  u32x2 kx;
  kv_load(P, kvg, va + 2048);
  asm volatile("global_load_dwordx4 %0, %5, %7 sc1\n\tglobal_load_dwordx4 %1, %5, %7 offset:36 sc1\n\tglobal_load_dword %2, %6, %7 offset:32 sc1\n\t"
               "global_load_dword %3, %6, %7 offset:68 sc1\n\tglobal_load_dwordx2 %4, %8, %9 sc1"
               : "=&v"(qa), "=&v"(qb), "=&v"(qa2), "=&v"(qb2), "=&v"(kx)
               : "v"(r * 1152 + 16 * h), "v"(r * 1152), "s"(ws + WS_Q + ((size_t)tb * 32 * D_ + 18 * hd) * 2), "v"(lane * 8), "s"(ws + WS_KMAX) : "memory");
  kv_load(R, kvg + KV_HS, va + 2048);
  lds_wait(lds0 + SY_MF, 4);
  asm volatile("s_waitcnt vmcnt(10)" : "+v"(P[0]), "+v"(P[1]), "+v"(P[2]), "+v"(P[3]), "+v"(P[4]) :: "memory");
  kv_put((lds_ptr)smem + va, P);
  asm volatile("s_waitcnt vmcnt(5)" : "+v"(qa), "+v"(qb), "+v"(qa2), "+v"(qb2), "+v"(kx) :: "memory");
  unsigned ku = max(kx[0], kx[1]);
  ku = max(ku, (unsigned)__builtin_amdgcn_update_dpp(0, (int)ku, 0xB1, 0xF, 0xF, true));
  ku = max(ku, (unsigned)__builtin_amdgcn_update_dpp(0, (int)ku, 0x4E, 0xF, 0xF, true));
  ku = max(ku, (unsigned)__builtin_amdgcn_update_dpp(0, (int)ku, 0x141, 0xF, 0xF, true));
  ku = max(ku, (unsigned)__builtin_amdgcn_update_dpp(0, (int)ku, 0x140, 0xF, 0xF, true));
  const float km = __uint_as_float(max(max((unsigned)__builtin_amdgcn_readlane(ku, 0), (unsigned)__builtin_amdgcn_readlane(ku, 16)),
                                       max((unsigned)__builtin_amdgcn_readlane(ku, 32), (unsigned)__builtin_amdgcn_readlane(ku, 48))));
  float na = 0.f, nb = 0.f;
#pragma unroll
  for (int j = 0; j < 4; ++j) {
    na += bflo(qa[j]) * bflo(qa[j]) + bfhi(qa[j]) * bfhi(qa[j]);
    nb += bflo(qb[j]) * bflo(qb[j]) + bfhi(qb[j]) * bfhi(qb[j]);
  }
  na = half_sum(na) + bflo(qa2) * bflo(qa2) + bfhi(qa2) * bfhi(qa2);
  nb = half_sum(nb) + bflo(qb2) * bflo(qb2) + bfhi(qb2) * bfhi(qb2);
  float ma = sqrtf(na * km) * 1.001f, mb = sqrtf(nb * km) * 1.001f;
  const bf16x8 qa1 = __builtin_bit_cast(bf16x8, qa), qb1 = __builtin_bit_cast(bf16x8, qb);
  const int xcd = u & 7, j3 = u >> 3, sp = xcd >> 1, tile = ((xcd & 1) * 8 + (j3 >> 2)) * 4 + (j3 & 3);
  char* box = ws + WS_P + (size_t)(tile * 4 + sp) * 28672 + w * 7168 + 3072;
  f32x16 oa, ob;
  float la, lb;
  int tgt = 0;
  PIN(ma); PIN(mb);
  KV_WAIT(R);
  for (int attempt = 0;; ++attempt) {
#pragma unroll
    for (int i = 0; i < 16; ++i) { oa[i] = 0.f; ob[i] = 0.f; }
    attn_main(ws, smem, wo, w2, attempt == 0 && w < 3 && u < 216 ? u * 3 + w : -1, u, box, tag, tgt, R, w, lane, qa1, q_tail(qa2, ma, h), qb1, q_tail(qb2, mb, h), oa, ob);
    la = __shfl(oa[10], r); lb = __shfl(ob[10], r);
    if (attempt == 0 && !(la >= 8.2718061e-25f && lb >= 8.2718061e-25f)) *(lds_word)(uintptr_t)(lds0 + SY_BAD) = 1;
    DRAIN();
    role_barrier(lds0 + SY_A, tgt);
    if (attempt || !__builtin_amdgcn_readfirstlane(lds_peek(lds0 + SY_BAD))) break;
    ma = -INFINITY; mb = -INFINITY;
    attn_max(smem, kvg, tgt, w, lane, qa1, q_tail(qa2, 0.f, h), qb1, q_tail(qb2, 0.f, h), ma, mb);
    kv_load(P, kvg, va + 2048);
    kv_load(R, kvg + KV_HS, va + 2048);
    KV_WAIT(P);
    KV_WAIT(R);
    kv_put((lds_ptr)smem + va, P);
  }
  __builtin_amdgcn_s_setprio(0);
  const char* wsrc = ws + WS_WP + (size_t)((j3 & 3) * 81 + sp * 20) * G2_STAGE + w * 18432;
#pragma unroll
  for (int i = 0; i < 18; ++i) glds16(wsrc + i * 1024, lane * 16, lds0 + w * 18432 + i * 1024);
  char* hp = ws + WS_H + (size_t)tb * NBLK * 1024;
  const u32x2 tails = {attn_store(hp, hd, lane, oa, la), attn_store(hp, hd + 1, lane, ob, lb)};
  if (lane < 32) store8_wt(hp + (320 + (hd >> 3)) * 1024 + (((hd >> 1) & 1) * 32 + lane) * 16 + ((hd >> 2) & 1) * 8, tails);
  DRAIN();
  if (lds_arrive_rtn(lds0 + SY_AE) == 3 && lane < 8) {
    const int rd = lane < 4 ? 6 + (tb >> 5) + 8 * (((tb >> 2) & 7) * 4 + lane) : 2 * (lane - 4) + (xcd & 1) + 8 * j3;
    store8_wt(ws + WS_IN1 + rd * 1024 + (lane < 4 ? IN_O + (u & 15) : IN_B + sp) * 8, tag);
  }
  STAMP_AT(1, 1);
}

DI float silu_mul(float g, float u) {
  const float e = __builtin_amdgcn_exp2f(g * -1.4426950408889634f);
  return g * __builtin_amdgcn_rcpf(1.f + e) * u;
}

constexpr int G1_W3 = 9216, G1_SLOT = G1_W3 + 9216, G1_RING = SY - 4 * G1_SLOT;
DI void glds16h(const void* sbase, unsigned voff, unsigned lds_addr) {
  asm volatile("s_mov_b32 m0, %0\n\ts_mov_b32 exec_hi, 0\n\tglobal_load_lds_dwordx4 %1, %2\n\ts_mov_b32 exec_hi, -1" ::"s"(lds_addr), "v"(voff), "s"(sbase) : "memory");
}
template <int OFF>
DI void gload16(u32x4& a, const void* sbase, unsigned voff) {
  asm volatile("global_load_dwordx4 %0, %1, %2 offset:%3 sc1" : "+v"(a) : "v"(voff), "s"(sbase), "i"(OFF) : "memory");
}

#define GPRIO 1
#define GLOW_A 21
#define GLOW_B 27
DI u32x2 gemm1_unit(char* smem, char* ws, const unsigned short* w1, const unsigned short* w3, u32x2 e, int g1, int tg, int u, int w, int lane) {
  const unsigned lds0 = (unsigned)(uintptr_t)smem, syg = lds0 + SY_G;
  const int j = u >> 3, ct = (u & 7) * 4 + (j & 3), rt = j >> 2;
  const char* wsrc = (const char*)((w >> 1) ? w3 : w1) + (w & 1) * 16 * 9216 + ct * 288;
  const unsigned wdst = lds0 + G1_RING + (w >> 1) * G1_W3 + (w & 1) * 4608;
  unsigned woff[5];
#pragma unroll
  for (int i = 0; i < 5; ++i) { const unsigned ch = i * 64 + (i < 4 ? lane : lane & 31), row = ch / 18u; woff[i] = (row ^ ((row >> 1) & 4u)) * 9216u + (ch - row * 18u) * 16u; }
  const char* xsrc = ws + WS_XN + (size_t)(rt * 4 + w) * 36864;
  const unsigned xoff = lane * 16;
  int sw = rt * 9 / 4, sx = sw;
  u32x4 xf[3][2];
#pragma unroll
  for (int i = 0; i < 3; ++i) { xf[i][0] = u32x4{0, 0, 0, 0}; xf[i][1] = u32x4{0, 0, 0, 0}; }
#define G1_WP(s, i) do { if ((i) < 4) glds16(wsrc + (size_t)sw * 294912, woff[i], wdst + (~(s) & 3) * G1_SLOT + (i) * 1024); \
    else { glds16h(wsrc + (size_t)sw * 294912, woff[4], wdst + (~(s) & 3) * G1_SLOT + 4096); sw = sw == 17 ? 0 : sw + 1; } } while (0)
#define G1_XP(set, i) do { if ((i) == 0) gload16<0>(xf[set][0], xsrc + sx * 2048, xoff); \
    else { gload16<1024>(xf[set][1], xsrc + sx * 2048, xoff); sx = sx == 17 ? 0 : sx + 1; } } while (0)
#pragma unroll
  for (int i = 0; i < 5; ++i) G1_WP(0, i);
  asm volatile("s_waitcnt vmcnt(5) lgkmcnt(0)\n\ts_barrier" : "+v"(e) :: "memory");
  const u32x2 tag = make_tag(e);
  STAMP_AT(1, 0);
#pragma unroll
  for (int i = 0; i < 5; ++i) G1_WP(1, i);
  if (w == 0) {
    lds_wait(lds0 + SY_XN, 4);
    if (lane < 32) store8_wt(ws + WS_IN1 + ((lane >> 2) + 8 * (((tg >> 1) & 7) * 4 + (lane & 3))) * 1024 + ((tg >> 4) * 16 + (tg & 1) * 8 + g1) * 8, tag);
  }
  STAMP_AT(1, 7);
  const char* inx = ws + WS_IN1 + u * 1024 + (lane & 31) * 8;
  const u64 xneed = 0xFFull << (8 * (w >> 1));
  const bool late = (poll_words(inx, xneed, tag) >> 16 & xneed) != xneed;
  G1_XP(0, 0); G1_XP(0, 1); G1_XP(1, 0); G1_XP(1, 1);
  lds_wait(lds0 + SY_MF, 4);
#pragma unroll
  for (int i = 0; i < 5; ++i) G1_WP(2, i);
  __builtin_amdgcn_s_setprio(GPRIO);
  const int c = lane & 15, q = lane >> 4;
  const int aoff0 = G1_RING + (8 * q + 4 * (q & 1) + (c >> 2)) * 288 + 8 * (c & 3), aoff1 = G1_RING + (8 * q + 4 * ((q & 1) ^ 1) + (c >> 2)) * 288 + 8 * (c & 3);
#define G1_RD(p, i) trread((p) + ((i) / 9) * G1_W3 + ((i) % 9) * 32)
  bf16x4 al[6], ah[6];
  STAMP_DECL;
  STAMP_R(2);
  lds_arrive(syg);
  lds_wait(syg, 4);
  STAMP_R(3);
#pragma unroll
  for (int i = 0; i < 6; ++i) { al[i] = G1_RD((lds_cptr)smem + 3 * G1_SLOT + aoff0, i); if (i < 5) ah[i] = G1_RD((lds_cptr)smem + 3 * G1_SLOT + aoff1, i); }
  int g = 0, seen = 0;
  f32x4 acc[36];
#define G1_STAGE(s, VM, LW, LX) do { \
    asm volatile("s_waitcnt vmcnt(" #VM ")" : "+v"(xf[s][0]), "+v"(xf[s][1]) :: "memory"); \
    lds_arrive(syg); \
    const lds_cptr pc = (lds_cptr)smem + (~g & 3) * G1_SLOT, pn = (lds_cptr)smem + (~(g + 1) & 3) * G1_SLOT; \
    const lds_cptr pc0 = pc + aoff0, pc1 = pc + aoff1, pn0 = pn + aoff0, pn1 = pn + aoff1; \
    SBAR(); \
    _Pragma("unroll") for (int i = 0; i < 18; ++i) { \
      MFMA16A(acc[2 * i], cat(al[i % 6], ah[i % 6]), xf[s][0]); \
      SBAR(); \
      if (i == 9) LDS_CHECK(seen, syg, 4 * g + 8); \
      ah[(i + 5) % 6] = i < 13 ? G1_RD(pc1, i + 5) : G1_RD(pn1, i - 13); \
      if (i == 4) seen = lds_peek(syg); \
      if (LW && (i & 1) && i >= 9) G1_WP(g + 3, (i - 9) >> 1); \
      if (LX && i == 1) { if (g == 16) xsrc += 32 * 36864; G1_XP((s + 2) % 3, 0); } \
      if (LX && i == 3) G1_XP((s + 2) % 3, 1); \
      SBAR(); \
      MFMA16A(acc[2 * i + 1], cat(al[i % 6], ah[i % 6]), xf[s][1]); \
      SBAR(); \
      al[i % 6] = i < 12 ? G1_RD(pc0, i + 6) : G1_RD(pn0, i - 12); \
      SBAR(); \
    } \
    ++g; } while (0)
  for (int un = 0; un < 2; ++un) {
#pragma unroll
    for (int b = 0; b < 36; ++b) acc[b] = f32x4{0.f, 0.f, 0.f, 0.f};
    for (int trip = un; trip < 6; ++trip) {
      if (__builtin_expect(late && g == 15, 0)) poll_words(inx, xneed << 16, tag);
      if (g == GLOW_A) __builtin_amdgcn_s_setprio(0);
      if (g == GLOW_B) __builtin_amdgcn_s_setprio(GPRIO);
      G1_STAGE(0, 7, true, true); G1_STAGE(1, 7, true, true); G1_STAGE(2, 7, true, true);
    }
    if (un) { G1_STAGE(0, 7, false, true); G1_STAGE(1, 2, false, false); G1_STAGE(2, 0, false, false); }
    MFMA_DRAIN();
    if (un == 0) STAMP_R(4); else STAMP_R(6);
    char* hp = ws + WS_H + ((size_t)(rt * 4 + un * 32 + w) * NBLK + ct * 9) * 1024 + ((q & 1) * 32 + (q >> 1) * 16 + c) * 16;
#pragma unroll
    for (int b = 0; b < 9; ++b) {
      PIN(acc[2 * b]); PIN(acc[2 * b + 1]); PIN(acc[18 + 2 * b]); PIN(acc[19 + 2 * b]);
      unsigned t[2][2];
#pragma unroll
      for (int i = 0; i < 4; ++i) {
        const f32x4 &ga = acc[2 * b + (i >> 1)], &ua = acc[18 + 2 * b + (i >> 1)];
        t[i >> 1][i & 1] = cvtpk(silu_mul(ga[2 * (i & 1)], ua[2 * (i & 1)]), silu_mul(ga[2 * (i & 1) + 1], ua[2 * (i & 1) + 1]));
      }
      const auto s0 = __builtin_amdgcn_permlane32_swap(t[0][0], t[1][0], false, false), s1 = __builtin_amdgcn_permlane32_swap(t[0][1], t[1][1], false, false);
      store16_wt(hp + b * 1024, (u32x4{s0[0], s1[0], s0[1], s1[1]}));
    }
    if (un == 0) STAMP_R(5);
  }
  __builtin_amdgcn_s_setprio(0);
  DRAIN();
  if (lds_arrive_rtn(lds0 + SY_GE) == 3 && lane < 16) {
    const int sp = min(3, (9 * ct + (lane & 8)) / 80);
    store8_wt(ws + WS_IN1 + (2 * sp + (lane & 1) + 8 * (rt * 4 + ((lane >> 1) & 3))) * 1024 + (IN_H + ct - sp * 80 / 9) * 8, tag);
  }
  STAMP_FLUSH(1, 2, 6);
  STAMP_AT(1, 1);
#undef G1_WP
#undef G1_XP
#undef G1_RD
#undef G1_STAGE
  return tag;
}

#define PSLEEP 1
DI void bload16(u32x4& a, __amdgpu_buffer_rsrc_t r, unsigned voff, unsigned soff) {
  asm volatile("buffer_load_dwordx4 %0, %1, %2, %3 offen sc1" : "=v"(a) : "v"(voff), "s"(r), "s"(soff) : "memory");
}
template <int OFF>
DI void blds16(__amdgpu_buffer_rsrc_t r, unsigned voff, unsigned soff) {
  asm volatile("buffer_load_dwordx4 %0, %1, %2 offen offset:%3 lds" ::"v"(voff), "s"(r), "s"(soff), "i"(OFF) : "memory");
}
template <int OFF>
DI void blds4(__amdgpu_buffer_rsrc_t r, unsigned voff, unsigned soff) {
  asm volatile("buffer_load_dword %0, %1, %2 offen offset:%3 lds" ::"v"(voff), "s"(r), "s"(soff), "i"(OFF) : "memory");
}
template <int ADD>
DI void set_m0(unsigned base) { asm volatile("s_add_u32 m0, %0, %1" ::"s"(base), "i"(ADD) : "memory", "scc"); }
template <int N>
DI void wait_bar(u32x4& a, u32x4& b) { asm volatile("s_waitcnt vmcnt(%2)\n\ts_barrier" : "+v"(a), "+v"(b) : "i"(N) : "memory"); }

template <int T>
DI void g2_iter(f32x4 (&acc)[2][9], bf16x8 (&af)[9], u32x4 (&hf)[6][2], const lds_cptr (&ap)[3], __amdgpu_buffer_rsrc_t rw, __amdgpu_buffer_rsrc_t rh,
                unsigned w16, unsigned w4, unsigned hoff, unsigned hoff1, unsigned wdst, unsigned h20) {
  constexpr int NH = T == 3 ? 3 : (T + 4 < 20 ? T + 4 : 20) - T, NW = T < 3 ? T + 2 : (T + 5 < 20 ? T + 5 : 20) - T - 1;
  wait_bar<(T == 20 ? 0 : 2 * NH + 3 * NW)>(hf[T % 6][0], hf[T % 6][1]);
  SBAR();
#pragma unroll
  for (int b = 0; b < 9; ++b) {
    MFMA16A(acc[0][b], af[b], hf[T % 6][0]);
    SBAR();
    if (T + 5 <= 20 && b == 0) bload16(hf[(T + 5) % 6][0], rh, hoff, T == 15 ? h20 : (T + 5) * 4096);
    if (T + 5 <= 20 && b == 1) bload16(hf[(T + 5) % 6][1], rh, hoff1, T == 15 ? h20 : (T + 5) * 4096);
    if (T + 6 <= 20 && b == 2) set_m0<((T + 6) & 7) * G2_STAGE>(wdst);
    if (T + 6 <= 20 && b == 3) blds16<0>(rw, w16, (T + 6) * G2_STAGE);
    if (T + 6 <= 20 && b == 5) blds16<1024>(rw, w16, (T + 6) * G2_STAGE);
    if (T + 6 <= 20 && b == 7) blds4<2048>(rw, w4, (T + 6) * G2_STAGE);
    SBAR();
    MFMA16A(acc[1][b], af[b], hf[T % 6][1]);
    SBAR();
    if (T < 20) af[b] = LDS128(ap[((T + 1) & 7) / 3] + (((T + 1) & 7) % 3) * G2_STAGE + b * 1024);
    SBAR();
  }
}
template <int T, int E, typename... A>
DI void g2_run(A&... a) { if constexpr (T < E) { g2_iter<T>(a...); g2_run<T + 1, E>(a...); } }

DI void gemm2_unit(char* smem, const unsigned short* x, char* ws, unsigned short* out, u32x2 tag, int u, int w, int lane) {
  const unsigned lds0 = (unsigned)(uintptr_t)smem;
  const int xcd = u & 7, j = u >> 3;
  const int sp = xcd >> 1, rt = (xcd & 1) * 8 + (j >> 2), ct = j & 3;
  const int wm = w & 3, kg = w >> 2;
  const int c = lane & 15, q = lane >> 4;
  STAMP_DECL;
  STAMP_R(0);
  const __amdgpu_buffer_rsrc_t rw = __builtin_amdgcn_make_buffer_rsrc(ws + WS_WP + (unsigned)__builtin_amdgcn_readfirstlane((ct * 81 + sp * 20) * G2_STAGE + w * 2304), 0, 21 * G2_STAGE, 0x00020000);
  const __amdgpu_buffer_rsrc_t rh = __builtin_amdgcn_make_buffer_rsrc(ws + WS_H + (unsigned)__builtin_amdgcn_readfirstlane((rt * 4 + wm) * NBLK * 1024 + sp * 81920 + kg * 2048), 0, 21 * 4096, 0x00020000);
  unsigned w16 = lane * 16, w4 = lane * 4, wdst = lds0 + w * 2304, h20 = sp == 3 ? 20 * 4096 : 19 * 4096;
  unsigned hoff = (q >> 1) * 1024 + (q & 1) * 512 + c * 16 + kg * 256, hoff1 = hoff ^ 256;
  u32x4 hf[6][2];
  const char* inh = ws + WS_IN1 + u * 1024 + (IN_H + min(lane, 35)) * 8;
  const bool late = (poll_words(inh, sp == 3 ? 0x3Full | (0xFull << (16 + 4 * wm)) : sp ? 0x3FFull : 0x1FFull, tag) >> 32 & 15) != 15;
  STAMP_R(4);
#pragma unroll
  for (int s = 0; s < 5; ++s) { bload16(hf[s][0], rh, hoff, s * 4096); bload16(hf[s][1], rh, hoff1, s * 4096); }
  asm volatile("s_barrier" ::: "memory");
#define G2_PW(s) do { set_m0<(s) * G2_STAGE>(wdst); asm volatile("s_nop 0"); blds16<0>(rw, w16, (s) * G2_STAGE); blds16<1024>(rw, w16, (s) * G2_STAGE); blds4<2048>(rw, w4, (s) * G2_STAGE); } while (0)
  G2_PW(4); G2_PW(5);
#undef G2_PW
  f32x4 acc[2][9];
#pragma unroll
  for (int i = 0; i < 2; ++i)
#pragma unroll
    for (int b = 0; b < 9; ++b) { acc[i][b] = f32x4{0.f, 0.f, 0.f, 0.f}; PIN(acc[i][b]); }
  lds_cptr ap[3];
#pragma unroll
  for (int i = 0; i < 3; ++i) { ap[i] = (lds_cptr)smem + i * 3 * G2_STAGE + kg * 9216 + lane * 16; PIN(ap[i]); }
  bf16x8 af[9];
#pragma unroll
  for (int b = 0; b < 9; ++b) af[b] = LDS128(ap[0] + b * 1024);
  g2_run<0, 20>(acc, af, hf, ap, rw, rh, w16, w4, hoff, hoff1, wdst, h20);
  if (sp == 3) g2_iter<20>(acc, af, hf, ap, rw, rh, w16, w4, hoff, hoff1, wdst, h20);
  STAMP_R(1);
  asm volatile("s_nop 7\n\ts_waitcnt vmcnt(0) lgkmcnt(0)\n\ts_barrier" : "+v"(hf[20 % 6][0]), "+v"(hf[20 % 6][1]) :: "memory");
#pragma unroll
  for (int b = 0; b < 9; ++b) { PIN(acc[0][b]); PIN(acc[1][b]); }
  f32x4* red = (f32x4*)(smem + (wm * 2) * 9216) + lane;
#pragma unroll
  for (int b = 0; b < 9; ++b) red[(kg ^ 1) * 576 + b * 64] = acc[1][b];
  if (late) poll_words(inh, 0xFull << 32, tag);
  BARRIER_LGKM();
  STAMP_R(7);
  const size_t orow = (size_t)(rt * 128 + wm * 32 + kg * 16) * D_ + ct * 144;
  const size_t oi = orow + (lane >> 2) * D_ + sp * 32 + (lane & 3) * 8, ot = orow + ((lane & 31) >> 1) * D_ + 128 + (lane & 1) * 8;
  const u32x4 xv = *(const u32x4*)(x + oi), xt = *(const u32x4*)(x + ot);
  f32x4 v[10];
#pragma unroll
  for (int b = 0; b < 9; ++b) v[b] = acc[0][b] + red[kg * 576 + b * 64];
#pragma unroll
  for (int i = 0; i < 4; ++i) v[9][i] = __uint_as_float(__builtin_amdgcn_permlane32_swap(__float_as_uint(v[8][i]), __float_as_uint(v[8][i]), false, false)[1]);
  char* box = ws + WS_P + (size_t)(rt * 4 + ct) * (4 * 28672) + lane * 16;
#define PK8(a, b) u32x4{min(cvtpk(a[0], a[1]), ~1u), min(cvtpk(a[2], a[3]), ~1u), min(cvtpk(b[0], b[1]), ~1u), min(cvtpk(b[2], b[3]), ~1u)}
#pragma unroll
  for (int j = 0; j < 4; ++j)
    if (j != sp) store16_wt(box + j * 28672 + ((sp - j - 1) & 3) * 9216 + w * 1024, PK8(v[2 * j], v[2 * j + 1]));
  if (wm != sp && lane < 32) store16_wt(box + wm * 28672 + ((sp - wm - 1) & 3) * 9216 + 8192 + kg * 512, PK8(v[8], v[9]));
#undef PK8
  f32x4 lo = v[0], hi = v[1];
#pragma unroll
  for (int j = 1; j < 4; ++j)
    if (j == sp) { lo = v[2 * j]; hi = v[2 * j + 1]; }
  const char* in = box + sp * 28672 + w * 1024;
  const char* tin = box + sp * 28672 + 8192 + kg * 512;
  u32x4 r[6];
  u64 m[6];
#pragma unroll
  for (int i = 0; i < 6; ++i) { r[i] = u32x4{0u, 0u, 0u, 0u}; m[i] = i < 3 ? ~0ull : wm == sp ? 0xFFFFFFFFull : 0ull; }
  STAMP_R(5);
#pragma nounroll
  for (int spin = 0; spin < GSPIN; ++spin) {
    asm volatile("s_mov_b64 exec, %12\n\tglobal_load_dwordx4 %0, %6, off sc1\n\ts_mov_b64 exec, %13\n\tglobal_load_dwordx4 %1, %7, off sc1\n\t"
                 "s_mov_b64 exec, %14\n\tglobal_load_dwordx4 %2, %8, off sc1\n\ts_mov_b64 exec, %15\n\tglobal_load_dwordx4 %3, %9, off sc1\n\t"
                 "s_mov_b64 exec, %16\n\tglobal_load_dwordx4 %4, %10, off sc1\n\ts_mov_b64 exec, %17\n\tglobal_load_dwordx4 %5, %11, off sc1\n\t"
                 "s_mov_b64 exec, -1\n\ts_waitcnt vmcnt(0)"
                 : "+v"(r[0]), "+v"(r[1]), "+v"(r[2]), "+v"(r[3]), "+v"(r[4]), "+v"(r[5])
                 : "v"(in), "v"(in + 9216), "v"(in + 18432), "v"(tin), "v"(tin + 9216), "v"(tin + 18432),
                   "s"(m[0]), "s"(m[1]), "s"(m[2]), "s"(m[3]), "s"(m[4]), "s"(m[5]) : "memory");
#pragma unroll
    for (int i = 0; i < 6; ++i) m[i] = __ballot(max(max(r[i][0], r[i][1]), max(r[i][2], r[i][3])) == ~0u);
    if (!(m[0] | m[1] | m[2] | m[3] | m[4] | m[5])) break;
    __builtin_amdgcn_s_sleep(PSLEEP);
  }
  STAMP_R(3);
#define FIN(o, xx, a, b, r0, src) _Pragma("unroll") for (int k = 0; k < 4; ++k) { \
    const float e = (k < 2 ? a[2 * k] : b[2 * k - 4]) + bflo(r[r0][k]) + (bflo(r[r0 + 1][k]) + bflo(r[r0 + 2][k])); \
    const float f = (k < 2 ? a[2 * k + 1] : b[2 * k - 3]) + bfhi(r[r0][k]) + (bfhi(r[r0 + 1][k]) + bfhi(r[r0 + 2][k])); \
    o[k] = cvtpk(bflo(xx[k]) + __shfl(e, src), bfhi(xx[k]) + __shfl(f, src)); }
  u32x4 o;
  FIN(o, xv, lo, hi, 0, (lane & 3) * 16 + (lane >> 2));
  *(u32x4*)(out + oi) = o;
  if (wm == sp) {
    FIN(o, xt, v[8], v[9], 3, (lane & 1) * 16 + ((lane & 31) >> 1));
    if (lane < 32) *(u32x4*)(out + ot) = o;
  }
#undef FIN
  STAMP_R(2);
  STAMP_FLUSH(2, 0, 7);
}

__global__ __launch_bounds__(512, 2) void fused_block(const unsigned short* __restrict__ x, const unsigned short* __restrict__ wq, const unsigned short* __restrict__ wk,
                                                      const unsigned short* __restrict__ wv, const unsigned short* __restrict__ wo, const unsigned short* __restrict__ w1,
                                                      const unsigned short* __restrict__ w3, const unsigned short* __restrict__ w2, char* __restrict__ ws,
                                                      unsigned short* __restrict__ out) {
  extern __shared__ __attribute__((aligned(16))) char smem[];
  const int lane = threadIdx.x & 63, u = blockIdx.x;
  const int w = __builtin_amdgcn_readfirstlane(threadIdx.x >> 6);
  const int g = (u >> 3) & 7, tg = (u & 7) * 4 + (u >> 6);
  u32x2 e, tag;
  asm volatile("global_load_dwordx2 %0, %1, %2 sc1" : "=v"(e) : "v"(0), "s"(ws + WS_E) : "memory");
  if (w >= 4) {
    if (w == 4 && lane < 16) ((lds_word)(smem + SY))[lane] = 0;
    p1_load(smem, wq, wk, wv, g, tg, w, lane);
    tag = gemm1_unit(smem, ws, w1, w3, e, g, tg, u, w - 4, lane);
  } else {
    __builtin_amdgcn_s_setprio(3);
    tag = p1_compute(smem, x, ws, g, tg, w, lane, e);
    attn_unit(smem, ws, wo, w2, tag, u, w, lane);
  }
  gemm2_unit(smem, x, ws, out, tag, u, w, lane);
  if (u == 0 && w == 0 && lane == 0) store8_wt(ws + WS_E, tag);
}

extern "C" void kernel_launch(void* const* d_in, const int* in_sizes, int n_in, void* d_out, int out_size, void* d_ws, size_t ws_size,
                              hipStream_t stream) {
  (void)hipFuncSetAttribute((const void*)fused_block, hipFuncAttributeMaxDynamicSharedMemorySize, LDS_ALL);
  fused_block<<<256, 512, LDS_ALL, stream>>>((const unsigned short*)d_in[0], (const unsigned short*)d_in[1], (const unsigned short*)d_in[2],
                                             (const unsigned short*)d_in[3], (const unsigned short*)d_in[4], (const unsigned short*)d_in[6],
                                             (const unsigned short*)d_in[7], (const unsigned short*)d_in[8], (char*)d_ws, (unsigned short*)d_out);
}
```

```cpp
#include <hip/hip_runtime.h>
#include <hip/hip_bf16.h>
#include <stdint.h>

typedef short bf16x8 __attribute__((ext_vector_type(8)));
typedef short bf16x4 __attribute__((ext_vector_type(4)));
typedef float f32x4 __attribute__((ext_vector_type(4)));
typedef float f32x16 __attribute__((ext_vector_type(16)));
typedef unsigned u32x4 __attribute__((ext_vector_type(4)));
typedef unsigned u32x2 __attribute__((ext_vector_type(2)));
typedef unsigned u32x4_a4 __attribute__((ext_vector_type(4), aligned(4)));
typedef __attribute__((address_space(3))) const char* lds_cptr;
typedef __attribute__((address_space(3))) char* lds_ptr;
#define DI __device__ __forceinline__

constexpr int S_ = 2048, D_ = 576, F_ = 4608;
constexpr int NBLK = 324;
constexpr int KV_HS = 20480;
constexpr int KV_K2 = 8192, KV_V = 10240;
constexpr size_t WS_XN = 0;
constexpr size_t WS_Q = WS_XN + (size_t)S_ * D_ * 2;
constexpr size_t WS_KV = WS_Q + (size_t)S_ * D_ * 2;
constexpr size_t WS_KMAX = WS_KV + 8 * KV_HS;
constexpr size_t WS_WP = WS_KMAX + 1024;
constexpr size_t WS_H = WS_WP + (size_t)4 * 162 * 9216;
constexpr size_t WS_P = WS_H + (size_t)64 * NBLK * 1024;
constexpr size_t WS_IN1 = WS_P + (size_t)256 * 28672;
constexpr size_t WS_PKF = WS_IN1 + 256 * 1024;
constexpr size_t WS_KVF = WS_PKF + 2048;
constexpr size_t WS_E = WS_KVF + 32 * 256;
constexpr int IN_Q = 64, IN_H = 80, IN_O = 96, IN_B = 112;
constexpr int SY = 163072, SY_XN = SY, SY_MF = SY + 4, SY_Q = SY + 8, SY_PKS = SY + 16, SY_A = SY + 20, SY_G = SY + 24, SY_BAD = SY + 28,
              SY_AE = SY + 32, SY_GE = SY + 36, SY_PX = SY + 40, SY_PK = SY + 44, SY_PH = SY + 48, SY_HE = SY + 52, SY_PQ = SY + 56, LDS_ALL = SY + 64;

DI unsigned cvtpk(float lo, float hi) {
  typedef __bf16 b2 __attribute__((ext_vector_type(2)));
  typedef float f2 __attribute__((ext_vector_type(2)));
  f2 v = {lo, hi};
  return __builtin_bit_cast(unsigned, __builtin_convertvector(v, b2));
}
DI float bflo(unsigned u) { return __uint_as_float(u << 16); }
DI float bfhi(unsigned u) { return __uint_as_float(u & 0xffff0000u); }
DI void glds16(const void* sbase, unsigned voff, unsigned lds_addr) {
  asm volatile("s_mov_b32 m0, %0\n\ts_nop 0\n\tglobal_load_lds_dwordx4 %1, %2" ::"s"(lds_addr), "v"(voff), "s"(sbase) : "memory");
}
DI void store16_wt(void* p, u32x4 v) {
  asm volatile("global_store_dwordx4 %0, %1, off sc1\n\ts_nop 1" ::"v"(p), "v"(v) : "memory");
}

typedef unsigned long long u64;
#define GSLEEP 4
#define GSPIN (1 << 20)
#define LSPIN (1 << 24)
DI void store8_wt(void* p, u32x2 v) { asm volatile("global_store_dwordx2 %0, %1, off sc1\n\ts_nop 1" ::"v"(p), "v"(v) : "memory"); }
DI void store4_wt(void* p, unsigned v) { asm volatile("global_store_dword %0, %1, off sc1\n\ts_nop 1" ::"v"(p), "v"(v) : "memory"); }
#define DRAIN() asm volatile("s_waitcnt vmcnt(0)" ::: "memory")
DI u64 poll_words(const char* p, u64 need, u32x2 tag) {
  u64 ok = 0;
#pragma nounroll
  for (int spin = 0; spin < GSPIN; ++spin) {
    u32x2 v;
    asm volatile("global_load_dwordx2 %0, %1, off sc1\n\ts_waitcnt vmcnt(0)" : "=v"(v) : "v"(p) : "memory");
    ok = __ballot(v[0] == tag[0] && v[1] == tag[1]);
    if ((ok & need) == need) break;
    __builtin_amdgcn_s_sleep(GSLEEP);
  }
  return ok;
}
typedef volatile __attribute__((address_space(3))) int* lds_word;
DI void lds_arrive(unsigned addr) {
  asm volatile("s_mov_b64 exec, 1\n\tds_add_u32 %0, %1\n\ts_mov_b64 exec, -1" ::"v"(addr), "v"(1u) : "memory");
}
DI int lds_arrive_rtn(unsigned addr) {
  int r;
  asm volatile("s_mov_b64 exec, 1\n\tds_add_rtn_u32 %0, %1, %2\n\ts_mov_b64 exec, -1\n\ts_waitcnt lgkmcnt(0)" : "=&v"(r) : "v"(addr), "v"(1u) : "memory");
  return __builtin_amdgcn_readfirstlane(r);
}
DI int lds_peek(unsigned addr) { return *(lds_word)(uintptr_t)addr; }
DI void lds_wait(unsigned addr, int target) {
#pragma nounroll
  for (int spin = 0; spin < LSPIN; ++spin) {
    if (__builtin_amdgcn_readfirstlane(lds_peek(addr)) >= target) break;
    __builtin_amdgcn_s_sleep(1);
  }
  asm volatile("" ::: "memory");
}
#define LDS_CHECK(v, addr, target) do { if (__builtin_expect(__builtin_amdgcn_readfirstlane(v) < (target), 0)) lds_wait(addr, target); asm volatile("" ::: "memory"); } while (0)

DI bf16x4 trread(lds_cptr p) {
  return __builtin_amdgcn_ds_read_tr16_b64_v4i16((__attribute__((address_space(3))) bf16x4*)p);
}
DI bf16x8 cat(bf16x4 a, bf16x4 b) { return __builtin_shufflevector(a, b, 0, 1, 2, 3, 4, 5, 6, 7); }
#define MFMA32(a, b, c) __builtin_amdgcn_mfma_f32_32x32x16_bf16((a), (b), (c), 0, 0, 0)
#define MFMA32K8(a, b, c) __builtin_amdgcn_mfma_f32_32x32x8bf16_1k((a), (b), (c), 0, 0, 0)
#define MFMA16(a, b, c) __builtin_amdgcn_mfma_f32_16x16x32_bf16((a), (b), (c), 0, 0, 0)
#define MFMA16A(c, a, b) asm volatile("v_mfma_f32_16x16x32_bf16 %0, %1, %2, %0" : "+v"(c) : "v"(a), "v"(b))
#define MFMA_DRAIN() asm volatile("s_nop 7\n\ts_nop 7" ::: "memory")
#define SBAR() __builtin_amdgcn_sched_barrier(0)
#define PIN(x) asm volatile("" : "+v"(x))
#define LDS128(p) (*(const __attribute__((address_space(3))) bf16x8*)(p))
#define LDS64(p) (*(const __attribute__((address_space(3))) bf16x4*)(p))
#define BARRIER_LGKM() asm volatile("s_waitcnt lgkmcnt(0)\n\ts_barrier" ::: "memory")

constexpr int K1_XT = 144 * 800, PK_BUF = 3 * KV_HS;
typedef float f32x2 __attribute__((ext_vector_type(2)));
typedef __attribute__((address_space(3))) u32x4* lds_v4;
DI float quad_sum(float v) {
  const auto a = __builtin_amdgcn_permlane32_swap(__float_as_uint(v), __float_as_uint(v), false, false);
  const float s = __uint_as_float(a[0]) + __uint_as_float(a[1]);
  const auto b = __builtin_amdgcn_permlane16_swap(__float_as_uint(s), __float_as_uint(s), false, false);
  return __uint_as_float(b[0]) + __uint_as_float(b[1]);
}
DI u32x2 make_tag(u32x2 e) {
  const u64 t = (((u64)(unsigned)__builtin_amdgcn_readfirstlane(e[1]) << 32) | (unsigned)__builtin_amdgcn_readfirstlane(e[0])) + 1;
  return u32x2{(unsigned)t, (unsigned)(t >> 32)};
}

DI void p1_load(char* smem, const unsigned short* wq, const unsigned short* wk, const unsigned short* wv, int g, int tg, int w, int lane) {
  const unsigned lds0 = (unsigned)(uintptr_t)smem;
  asm volatile("s_waitcnt lgkmcnt(0)\n\ts_barrier" ::: "memory");
  if (g == 7) {
    u32x4 a[3][6];
    unsigned b[3][2];
#pragma unroll
    for (int i = 0; i < 3; ++i) {
      const int k = min((w - 4) * 64 + lane + 256 * i, 575);
      if (i < 2 || w == 4) {
        const u32x4* qp = (const u32x4*)(wq + (size_t)k * D_ + 560);
        const unsigned* kp = (const unsigned*)(wk + k * 18);
        const unsigned* vp = (const unsigned*)(wv + k * 18);
        a[i][0] = qp[0]; a[i][1] = qp[1];
        a[i][2] = *(const u32x4_a4*)kp; a[i][3] = *(const u32x4_a4*)(kp + 4); b[i][0] = kp[8];
        a[i][4] = *(const u32x4_a4*)vp; a[i][5] = *(const u32x4_a4*)(vp + 4); b[i][1] = vp[8];
      }
    }
    asm volatile("" ::: "memory");
#pragma unroll
    for (int i = 0; i < 3; ++i) {
      const int k = (w - 4) * 64 + lane + 256 * i;
      if (i < 2 || w == 4) {
        const lds_ptr d = (lds_ptr)smem + ((k >> 2) ^ ((k >> 3) & 1)) * 800 + (k & 3) * 192;
        *(lds_v4)(d) = a[i][0]; *(lds_v4)(d + 16) = a[i][1]; *(lds_v4)(d + 32) = a[i][2]; *(lds_v4)(d + 48) = a[i][3];
        *(lds_v4)(d + 96) = a[i][4]; *(lds_v4)(d + 112) = a[i][5];
        *(__attribute__((address_space(3))) unsigned*)(d + 64) = b[i][0]; *(__attribute__((address_space(3))) unsigned*)(d + 128) = b[i][1];
      }
    }
  } else if (lane < 48) {
    const unsigned row = lane / 12u, voff = row * 1152u + (lane - row * 12u) * 16u;
    const char* base = (const char*)wq + ((160 * g) & ~63) + ((w & 3) ^ ((w >> 1) & 1)) * 4608;
    int u = (tg & 3) * 9;
#pragma unroll 4
    for (int i = 0; i < 36; ++i) {
      glds16(base + u * 18432, voff, lds0 + (w & 3) * 800 + u * 3200);
      u = u == 35 ? 0 : u + 1;
    }
  }
}
DI void pack_load(char* smem, const unsigned short* wo, const unsigned short* w2, int job, int w, int lane) {
  const int j = job >> 2;
  const unsigned ra = j < 144 ? 16 : j < 160 ? 18 : 144, rb = j < 160 ? 2 : 18, rc = j < 144 ? 32 * j : j < 160 ? 36 * (j - 144) : 288 * (j - 160) + 16;
  const char* base = (const char*)(j < 144 ? w2 : wo) + (size_t)rc * 1152 + (job & 3) * 288;
#pragma unroll
  for (int v = 0; v < 9; ++v) {
    const unsigned ch = v * 64 + lane, row = ch / 18u, cc = ch - row * 18u;
    const unsigned src = ra * (row >> 4) + rb * ((row & 15) >> 1) + (row & 1);
    glds16(base, src * 1152u + cc * 16u, (unsigned)(uintptr_t)smem + PK_BUF + w * 9216 + v * 1024);
  }
}
DI void pack_store(char* smem, char* ws, int job, int w, int lane) {
  const int c = lane & 15, q = lane >> 4;
  const lds_cptr pk = (lds_cptr)smem + PK_BUF + w * 9216 + (16 * (q >> 1) + 4 * (q & 1) + (c >> 2)) * 288;
  char* wp = ws + WS_WP + (size_t)((job & 3) * 162 + (job >> 2)) * 9216 + lane * 16;
#pragma unroll
  for (int b = 0; b < 9; ++b) {
    const lds_cptr a = pk + (b < 8 ? 64 * (b >> 1) + 8 * (b & 1) + 16 * (c & 3) : 256 + 16 * (c & 1) + 8 * ((c >> 1) & 1));
    store16_wt(wp + b * 1024, __builtin_bit_cast(u32x4, cat(trread(a), trread(a + 8 * 288))));
  }
}

DI u32x2 p1_compute(char* smem, const unsigned short* x, char* ws, int g, int tg, int w, int lane, u32x2 e) {
  const unsigned lds0 = (unsigned)(uintptr_t)smem;
  const int c = lane & 15, q = lane >> 4;
  const int tok = tg * 64 + w * 16 + c;
  u32x4 xr[18];
  {
    const char* xp = (const char*)x + (tg * 64 + w * 16 + (c & 12)) * 1152 + q * 64 + (c & 3) * 16;
#pragma unroll
    for (int i = 0; i < 16; ++i) xr[i] = *(const u32x4*)(xp + (i & 3) * 1152 + (i >> 2) * 256);
#pragma unroll
    for (int i = 16; i < 18; ++i) xr[i] = *(const u32x4*)((const char*)x + tok * 1152 + i * 64 + q * 16);
    asm volatile("s_barrier" ::: "memory");
    const lds_ptr tw = (lds_ptr)smem + K1_XT + w * 4352 + (c & 12) * 272 + q * 64 + (c & 3) * 16, tr = (lds_ptr)smem + K1_XT + w * 4352 + c * 272 + q * 16;
#pragma unroll
    for (int gq = 0; gq < 4; ++gq) {
#pragma unroll
      for (int i = 0; i < 4; ++i) *(lds_v4)(tw + i * 272) = xr[4 * gq + i];
      asm volatile("" ::: "memory");
#pragma unroll
      for (int i = 0; i < 4; ++i) xr[4 * gq + i] = *(lds_v4)(tr + i * 64);
      asm volatile("" ::: "memory");
    }
  }
  f32x2 d[72];
  f32x2 s2[2] = {{0.f, 0.f}, {0.f, 0.f}};
#pragma unroll
  for (int i = 0; i < 72; ++i) { d[i] = f32x2{bflo(xr[i >> 2][i & 3]), bfhi(xr[i >> 2][i & 3])}; s2[i & 1] += d[i]; }
  const float mean = quad_sum((s2[0][0] + s2[0][1]) + (s2[1][0] + s2[1][1])) * (1.f / 576.f);
  f32x2 v2[2] = {{0.f, 0.f}, {0.f, 0.f}};
#pragma unroll
  for (int i = 0; i < 72; ++i) { d[i] -= mean; v2[i & 1] += d[i] * d[i]; }
  const float rstd = 1.f / sqrtf(quad_sum((v2[0][0] + v2[0][1]) + (v2[1][0] + v2[1][1])) * (1.f / 576.f) + 1e-5f);
#pragma unroll
  for (int i = 0; i < 72; ++i) { const f32x2 y = d[i] * rstd; xr[i >> 2][i & 3] = cvtpk(y[0], y[1]); }
  {
    char* xn = ws + WS_XN + (size_t)(tok >> 5) * 36864 + (w & 1) * 1024 + lane * 16;
#pragma unroll
    for (int i = 0; i < 18; ++i)
      if ((i & 7) == g) store16_wt(xn + i * 2048, xr[i]);
  }
  asm volatile("s_barrier" ::: "memory");
  f32x4 acc[5];
#pragma unroll
  for (int b = 0; b < 5; ++b) acc[b] = f32x4{0.f, 0.f, 0.f, 0.f};
  const int co = (c >> 2) * 192 + 8 * (c & 3) + (g < 7 ? (g & 1) * 32 : 0);
  const lds_cptr p0 = (lds_cptr)smem + (2 * q + (q & 1)) * 800 + co, p1 = (lds_cptr)smem + (2 * q + 1 - (q & 1)) * 800 + co;
  const lds_cptr p2 = p0 + 57600, p3 = p1 + 57600;
  bf16x4 al[10], ah[10];
#define XNAT 40
#define K1_RD(n) do { al[(n) % 10] = trread(((n) < 45 ? p0 : p2) + ((n) % 45 / 5) * 6400 + ((n) % 5) * 32); \
                      ah[(n) % 10] = trread(((n) < 45 ? p1 : p3) + ((n) % 45 / 5) * 6400 + ((n) % 5) * 32); } while (0)
#define K1_XN(n) if ((n) == XNAT) { asm volatile("s_waitcnt vmcnt(0)" : "+v"(e) :: "memory"); lds_arrive(lds0 + SY_XN); }
#pragma unroll
  for (int n = 0; n < 9; ++n) K1_RD(n);
  if (g < 7) {
#pragma unroll
    for (int n = 0; n < 90; ++n) {
      if (n + 9 < 90) K1_RD(n + 9);
      acc[n % 5] = MFMA16(cat(al[n % 10], ah[n % 10]), __builtin_bit_cast(bf16x8, xr[n / 5]), acc[n % 5]);
      K1_XN(n);
      SBAR();
    }
  } else {
#pragma unroll
    for (int n = 0; n < 90; ++n) {
      if (n + 9 < 90) K1_RD(n + 9);
      if (n % 5 < 3) acc[n % 5] = MFMA16(cat(al[n % 10], ah[n % 10]), __builtin_bit_cast(bf16x8, xr[n / 5]), acc[n % 5]);
      else acc[n % 5] = MFMA16(__builtin_bit_cast(bf16x8, xr[n / 5]), cat(al[n % 10], ah[n % 10]), acc[n % 5]);
      K1_XN(n);
      SBAR();
    }
  }
#undef K1_RD
#undef K1_XN
  lds_arrive(lds0 + SY_MF);
  constexpr float QSCALE = 0.23570226039551584f * 1.4426950408889634f;
  char* qp = ws + WS_Q + ((size_t)tok * D_ + 80 * g) * 2;
#define QPK(b) u32x2{cvtpk(acc[b][0] * QSCALE, acc[b][1] * QSCALE), cvtpk(acc[b][2] * QSCALE, acc[b][3] * QSCALE)}
  if (g < 7) {
#pragma unroll
    for (int b = 0; b < 4; b += 2) {
      const u32x2 ua = QPK(b), ub = QPK(b + 1);
      const auto s0 = __builtin_amdgcn_permlane16_swap(ua[0], ub[0], false, false), s1 = __builtin_amdgcn_permlane16_swap(ua[1], ub[1], false, false);
      store16_wt(qp + b * 32 + (q & 1) * 32 + (q >> 1) * 16, u32x4{s0[0], s1[0], s0[1], s1[1]});
    }
    store8_wt(qp + 128 + q * 8, QPK(4));
  } else {
    store8_wt(qp + q * 8, QPK(0));
    char* kv = ws + WS_KV + (tok >> 8) * KV_HS;
    const int kk = tok & 255;
    const u32x2 kA = {cvtpk(acc[1][0], acc[1][1]), cvtpk(acc[1][2], acc[1][3])};
    const unsigned kB = cvtpk(acc[2][0], acc[2][1]);
    store8_wt(kv + (kk >> 5) * 1024 + ((q >> 1) * 32 + (kk & 31)) * 16 + (q & 1) * 8, kA);
    if (q == 0) store8_wt(kv + KV_K2 + kk * 8, u32x2{kB, 0x3F803F80u});
    const int t0 = (tg * 64 + w * 16) & 255;
    char* vp = kv + KV_V + (t0 >> 4) * 624 + (q & 1) * 304 + (q >> 1) * 8;
    store8_wt(vp + c * 16, u32x2{cvtpk(acc[3][0], acc[3][1]), cvtpk(acc[3][2], acc[3][3])});
    if (c < 3 || (c == 3 && (q & 1)))
      store8_wt(vp + (16 + c) * 16, c < 2 ? u32x2{cvtpk(acc[4][0], acc[4][1]), cvtpk(acc[4][2], acc[4][3])} : c == 2 ? u32x2{0x3F803F80u, 0x3F803F80u} : u32x2{0u, 0u});
    float n2 = bflo(kA[0]) * bflo(kA[0]) + bfhi(kA[0]) * bfhi(kA[0]) + bflo(kA[1]) * bflo(kA[1]) + bfhi(kA[1]) * bfhi(kA[1]);
    if (q == 0) n2 += bflo(kB) * bflo(kB) + bfhi(kB) * bfhi(kB);
    n2 = quad_sum(n2);
    n2 = fmaxf(n2, __shfl_xor(n2, 1)); n2 = fmaxf(n2, __shfl_xor(n2, 2));
    n2 = fmaxf(n2, __shfl_xor(n2, 4)); n2 = fmaxf(n2, __shfl_xor(n2, 8));
    if (lane == 0) store4_wt(ws + WS_KMAX + (tg * 4 + w) * 4, __float_as_uint(n2));
  }
#undef QPK
  const u32x2 tag = make_tag(e);
  DRAIN();
  if (lds_arrive_rtn(lds0 + SY_Q) == 3) {
    if (lane < 8 || (g == 7 && lane >= 32))
      store8_wt(ws + (lane < 8 ? WS_IN1 + (tg * 8 + lane) * 1024 + (IN_Q + g) * 8 : WS_KVF + (lane - 32) * 256 + tg * 8), tag);
  }
  return tag;
}

#define KV_LOADS "global_load_dwordx4 %0, %5, %6 offset:-2048 sc1\n\tglobal_load_dwordx4 %1, %5, %6 offset:-1024 sc1\n\tglobal_load_dwordx4 %2, %5, %6 sc1\n\t" \
                 "global_load_dwordx4 %3, %5, %6 offset:1024 sc1\n\tglobal_load_dwordx4 %4, %5, %6 offset:2048 sc1\n"
DI void kv_load(u32x4 (&R)[5], const char* src, unsigned va) {
  asm volatile(KV_LOADS : "=&v"(R[0]), "=&v"(R[1]), "=&v"(R[2]), "=&v"(R[3]), "=&v"(R[4]) : "v"(va), "s"(src) : "memory");
}
DI void kv_turn(u32x4 (&R)[5], int hs, const char* src, unsigned va, const char* in1, char* box, unsigned l16, unsigned l32, unsigned lh, u32x4 ones) {
  asm volatile("s_cmp_lt_u32 %7, 6\n\ts_cbranch_scc0 1f\n\t" KV_LOADS "\ts_branch 2f\n"
               "1:\n\ts_cmp_lg_u32 %7, 6\n\ts_cbranch_scc1 2f\n\t"
               "global_load_dwordx4 %0, %11, %8 sc1\n\tglobal_load_dwordx4 %1, %11, %8 offset:16 sc1\n\tglobal_load_dwordx4 %2, %13, %8 sc1\n\t"
               "global_store_dwordx4 %10, %12, %9 offset:-3072 sc1\n\tglobal_store_dwordx4 %10, %12, %9 offset:-2048 sc1\n\t"
               "global_store_dwordx4 %10, %12, %9 offset:-1024 sc1\n\tglobal_store_dwordx4 %10, %12, %9 sc1\n\t"
               "global_store_dwordx4 %10, %12, %9 offset:1024 sc1\n\tglobal_store_dwordx4 %10, %12, %9 offset:2048 sc1\n\t"
               "global_store_dwordx4 %10, %12, %9 offset:3072 sc1\n"
               "2:"
               : "+v"(R[0]), "+v"(R[1]), "+v"(R[2]), "+v"(R[3]), "+v"(R[4])
               : "v"(va), "s"(src), "s"(hs), "s"(in1), "s"(box), "v"(l16), "v"(l32), "v"(ones), "v"(lh) : "memory", "scc");
}
#undef KV_LOADS
#define KV_WAIT(R) asm volatile("s_waitcnt vmcnt(0)" : "+v"(R[0]), "+v"(R[1]), "+v"(R[2]), "+v"(R[3]), "+v"(R[4]) :: "memory")
DI void kv_put(lds_ptr d, const u32x4 (&R)[5]) {
#pragma unroll
  for (int i = 0; i < 5; ++i) *(lds_v4)(d + i * 1024) = R[i];
}
DI void role_barrier(unsigned addr, int& tgt) {
  asm volatile("s_waitcnt lgkmcnt(0)" ::: "memory");
  lds_arrive(addr);
  tgt += 4;
  lds_wait(addr, tgt);
}
DI bf16x4 q_tail(unsigned q2, float m, int h) {
  const float m1 = bflo(cvtpk(m, 0.f)), r1 = m - m1, m2 = bflo(cvtpk(r1, 0.f)), m3 = r1 - m2;
  return __builtin_bit_cast(bf16x4, h ? u32x2{0u, cvtpk(-m3, 0.f)} : u32x2{q2, cvtpk(-m1, -m2)});
}

#define ROT (u >> 3)
#define CODE2 8192
#define RSLEEP 3
#define APRIO_HI 2
#define APRIO_LO 0
DI void attn_main(char* ws, char* smem, const unsigned short* wo, const unsigned short* w2, int job, int u, char* box, u32x2 tag, int& tgt, u32x4 (&R)[5], int w, int lane, bf16x8 qa1, bf16x4 qa2, bf16x8 qb1, bf16x4 qb2,
                  f32x16& oa, f32x16& ob) {
  const unsigned sya = (unsigned)(uintptr_t)smem + SY_A;
  const char* kvg = ws + WS_KV;
  const int r = lane & 31, h = lane >> 5;
  const unsigned va = w * 5120 + lane * 16;
  const int ok1 = lane * 16, ok2 = KV_K2 + r * 8, ov = KV_V + (r < 19 ? h * 304 + r * 16 : 608);
  const f32x16 zero = {};
  u32x4 ones = {~0u, ~0u, ~0u, ~0u};
  PIN(ones);
  role_barrier(sya, tgt);
  bf16x8 k1 = LDS128((lds_cptr)smem + ok1), v0 = LDS128((lds_cptr)smem + ov), v1 = {};
  bf16x4 k2 = LDS64((lds_cptr)smem + ok2);
  f32x16 sa = MFMA32(k1, qa1, MFMA32K8(k2, qa2, zero)), sb;
  u32x4 p0, p1 = {0u, 0u, 0u, 0u};
  float e2 = 0.f, e3 = 0.f;
#define SLOT(MF, RD, NEXT, S, i, PP, jp, P, j) do { \
    MF; SBAR(); __builtin_amdgcn_s_setprio(APRIO_LO); SBAR(); \
    float e0 = __builtin_amdgcn_exp2f(S[i]); PIN(e0); SBAR(); RD; SBAR(); \
    float e1 = __builtin_amdgcn_exp2f(S[i + 1]); PIN(e1); SBAR(); \
    PP[jp] = cvtpk(e2, e3); PIN(PP); SBAR(); \
    e2 = __builtin_amdgcn_exp2f(S[i + 2]); PIN(e2); SBAR(); PIN(NEXT); SBAR(); \
    e3 = __builtin_amdgcn_exp2f(S[i + 3]); PIN(e3); SBAR(); __builtin_amdgcn_s_setprio(APRIO_HI); SBAR(); \
    P[j] = cvtpk(e0, e1); PIN(P); SBAR(); } while (0)
#define PB(x) __builtin_bit_cast(bf16x8, x)
  int c0 = 0, c1 = KV_HS, c2 = 2 * KV_HS, seen = 0;
  for (int hs = 0; hs < 8; ++hs) {
    const lds_cptr cur = (lds_cptr)smem + c0, nxt = (lds_cptr)smem + c1;
    const lds_cptr ck1 = cur + ok1, ck2 = cur + ok2, cv = cur + ov, nk1 = nxt + ok1, nk2 = nxt + ok2, nv = nxt + ov;
#pragma unroll
    for (int t = 0; t < 8; ++t) {
      if (t == 1) {
        KV_WAIT(R);
        kv_put((lds_ptr)smem + c1 + va, R);
        if (hs == 2 && job >= 0) pack_store(smem, ws, job, w, lane);
        if (hs == 3 && lds_arrive_rtn(sya - SY_A + SY_PKS) == 3 && lane == 0) store8_wt(ws + WS_PKF + u * 8, tag);
        if (hs == 7 && w == 0) {
          bool ok = true;
#pragma unroll
          for (int i = 0; i < 12; ++i) ok &= R[i >> 2][i & 3] == tag[i & 1];
          if (__builtin_expect(!__all(ok), 0))
#pragma nounroll
            for (int i = 0; i < 5; ++i) poll_words(ws + (i < 4 ? WS_PKF + i * 512 + lane * 8 : WS_IN1 + u * 1024 + (IN_H + (lane & 3)) * 8), ~0ull, tag);
          lds_arrive(sya - SY_A + SY_PH);
          asm volatile("buffer_inv sc1" ::: "memory");
        }
      }
      if (t == 2) {
        lds_arrive(sya);
        tgt += 4;
        kv_turn(R, hs, kvg + ((hs + 2 + ROT) & 7) * KV_HS, va + 2048, ws + WS_IN1, box, ok1, (unsigned)(WS_PKF - WS_IN1) + lane * 32, u * 1024 + IN_H * 8 + (lane & 1) * 16, ones);
      }
      if (t == 3 && hs == 1 && job >= 0) pack_load(smem, wo, w2, job, w, lane);
      if (t == 6) seen = lds_peek(sya);
      if (t == 7) LDS_CHECK(seen, sya, tgt);
      SBAR();
      SLOT(sb = MFMA32K8(k2, qb2, zero), k2 = t < 7 ? LDS64(ck2 + (t + 1) * 256) : LDS64(nk2), e0, sa, 0, p1, 3, p0, 0);
      SLOT(ob = MFMA32(v1, PB(p1), ob), v1 = LDS128(cv + (2 * t + 1) * 624), e0, sa, 4, p0, 1, p0, 2);
      SLOT(sb = MFMA32(k1, qb1, sb), k1 = t < 7 ? LDS128(ck1 + (t + 1) * 1024) : LDS128(nk1), v0, sa, 8, p0, 3, p1, 0);
      SLOT(oa = MFMA32(v0, PB(p0), oa), , k2, sa, 12, p1, 1, p1, 2);
      SLOT(sa = MFMA32K8(k2, qa2, zero), , v1, sb, 0, p1, 3, p0, 0);
      SLOT(oa = MFMA32(v1, PB(p1), oa), , k1, sb, 4, p0, 1, p0, 2);
      SLOT(sa = MFMA32(k1, qa1, sa), , e0, sb, 8, p0, 3, p1, 0);
      SLOT(ob = MFMA32(v0, PB(p0), ob), v0 = t < 7 ? LDS128(cv + (2 * t + 2) * 624) : LDS128(nv), e0, sb, 12, p1, 1, p1, 2);
    }
    const int cc = c0; c0 = c1; c1 = c2; c2 = cc;
  }
#undef SLOT
  p1[3] = cvtpk(e2, e3);
  ob = MFMA32(v1, PB(p1), ob);
#undef PB
}

DI void attn_max(char* smem, const char* kvg, int& tgt, int w, int lane, bf16x8 qa1, bf16x4 qa2, bf16x8 qb1, bf16x4 qb2, float& ma, float& mb) {
  const unsigned sya = (unsigned)(uintptr_t)smem + SY_A;
  const unsigned va = w * 5120 + lane * 16;
  const f32x16 zero = {};
  for (int hs = 0; hs < 8; ++hs) {
    u32x4 R[5];
    kv_load(R, kvg + hs * KV_HS, va + 2048);
    KV_WAIT(R);
    kv_put((lds_ptr)smem + va, R);
    role_barrier(sya, tgt);
#pragma unroll 1
    for (int t = 0; t < 8; ++t) {
      const bf16x8 k1 = LDS128((lds_cptr)smem + lane * 16 + t * 1024);
      const bf16x4 k2 = LDS64((lds_cptr)smem + KV_K2 + (lane & 31) * 8 + t * 256);
      const f32x16 sa = MFMA32(k1, qa1, MFMA32K8(k2, qa2, zero)), sb = MFMA32(k1, qb1, MFMA32K8(k2, qb2, zero));
#pragma unroll
      for (int i = 0; i < 16; ++i) { ma = fmaxf(ma, sa[i]); mb = fmaxf(mb, sb[i]); }
    }
    role_barrier(sya, tgt);
  }
  ma = fmaxf(ma, __shfl_xor(ma, 32));
  mb = fmaxf(mb, __shfl_xor(mb, 32));
}

DI unsigned attn_store(char* hp, int hd, int lane, const f32x16& o, float l) {
  const float inv = 1.f / l;
  store16_wt(hp + (288 + hd) * 1024 + lane * 16,
             (u32x4{cvtpk(o[0] * inv, o[1] * inv), cvtpk(o[2] * inv, o[3] * inv), cvtpk(o[4] * inv, o[5] * inv), cvtpk(o[6] * inv, o[7] * inv)}));
  return cvtpk(o[8] * inv, o[9] * inv);
}

DI float half_sum(float v) {
  const auto t = __builtin_amdgcn_permlane32_swap(__float_as_uint(v), __float_as_uint(v), false, false);
  return __uint_as_float(t[0]) + __uint_as_float(t[1]);
}
constexpr int G2_STAGE = 18432;
DI void bload16(u32x4& a, __amdgpu_buffer_rsrc_t r, unsigned voff, unsigned soff) {
  asm volatile("buffer_load_dwordx4 %0, %1, %2, %3 offen sc1" : "=v"(a) : "v"(voff), "s"(r), "s"(soff) : "memory");
}
DI __amdgpu_buffer_rsrc_t h_rsrc(char* ws, int u, int w) {
  const int rt = (u & 1) * 8 + (u >> 5), sp = (u & 7) >> 1;
  return __builtin_amdgcn_make_buffer_rsrc(ws + WS_H + (unsigned)__builtin_amdgcn_readfirstlane((rt * 4 + (w & 3)) * NBLK * 1024 + sp * 81920 + (w >> 2) * 2048), 0, 21 * 4096, 0x00020000);
}
DI unsigned h_off(int w, int lane) { return (lane >> 5) * 1024 + ((lane >> 4) & 1) * 512 + (lane & 15) * 16 + (w >> 2) * 256; }
#define H_FIRST(C) do { \
  const __amdgpu_buffer_rsrc_t rh_ = h_rsrc(ws, u, w8); \
  const unsigned ho_ = h_off(w8, lane); \
  _Pragma("unroll") for (int s_ = 0; s_ < 3; ++s_) \
    asm volatile("s_cmp_eq_u32 %6, 0\n\ts_cbranch_scc1 1f\n\tbuffer_load_dwordx4 %0, %2, %4, %5 offen sc1\n\tbuffer_load_dwordx4 %1, %3, %4, %5 offen sc1\n1:" \
                 : C(hf[s_][0]), C(hf[s_][1]) : "v"(ho_), "v"(ho_ ^ 256), "s"(rh_), "s"(s_ * 4096), "s"(go) : "memory", "scc"); } while (0)
DI void h_first(u32x4 (&hf)[6][2], char* ws, int u, int w8, int lane, int go) { H_FIRST("=&v"); }
DI void h_again(u32x4 (&hf)[6][2], char* ws, int u, int w8, int lane, int go) { H_FIRST("+v"); }
#undef H_FIRST
#define DRAIN_H(hf) asm volatile("s_waitcnt vmcnt(0)" : "+v"(hf[0][0]), "+v"(hf[0][1]), "+v"(hf[1][0]), "+v"(hf[1][1]), "+v"(hf[2][0]), "+v"(hf[2][1]) :: "memory")
DI void attn_unit(char* smem, char* ws, const unsigned short* wo, const unsigned short* w2, u32x2 tag, u32x4 (&hf)[6][2], u32x2& fl, int u, int w, int lane) {
  const unsigned lds0 = (unsigned)(uintptr_t)smem;
  const char* kvg = ws + WS_KV;
  const int gw = u * 4 + w, hd = (gw & 15) * 2, tb = gw >> 4;
  const int r = lane & 31, h = lane >> 5;
  const unsigned va = w * 5120 + lane * 16;
  const char* words = ws + (lane < 32 ? WS_KVF + (u & 31) * 256 + lane * 8 : WS_IN1 + u * 1024 + (IN_Q + (lane & 7)) * 8);
  u64 seen = 0;
  if (w == 0) {
    seen = poll_words(words, (0xE0380E03ull >> (8 * (u & 3)) & 0xFF) << 32, tag);
    lds_arrive(lds0 + SY_PQ);
  } else {
    lds_wait(lds0 + SY_PQ, 1);
  }
  u32x4 P[5], R[5], qa, qb;
  unsigned qa2, qb2;
  u32x2 kx;
  asm volatile("global_load_dwordx4 %0, %4, %6 sc1\n\tglobal_load_dwordx4 %1, %4, %6 offset:36 sc1\n\tglobal_load_dword %2, %5, %6 offset:32 sc1\n\t"
               "global_load_dword %3, %5, %6 offset:68 sc1"
               : "=&v"(qa), "=&v"(qb), "=&v"(qa2), "=&v"(qb2)
               : "v"(r * 1152 + 16 * h), "v"(r * 1152), "s"(ws + WS_Q + ((size_t)tb * 32 * D_ + 18 * hd) * 2) : "memory");
  if (w == 0) {
    if ((unsigned)seen != ~0u) poll_words(words, 0xFFFFFFFFull, tag);
    lds_arrive(lds0 + SY_PK);
  } else {
    lds_wait(lds0 + SY_PK, 1);
  }
  asm volatile("global_load_dwordx2 %0, %1, %2 sc1" : "=&v"(kx) : "v"(lane * 8), "s"(ws + WS_KMAX) : "memory");
  kv_load(P, kvg + (ROT & 7) * KV_HS, va + 2048);
  asm volatile("s_waitcnt vmcnt(6)" : "+v"(qa), "+v"(qb), "+v"(qa2), "+v"(qb2) :: "memory");
  float na = 0.f, nb = 0.f;
#pragma unroll
  for (int j = 0; j < 4; ++j) {
    na += bflo(qa[j]) * bflo(qa[j]) + bfhi(qa[j]) * bfhi(qa[j]);
    nb += bflo(qb[j]) * bflo(qb[j]) + bfhi(qb[j]) * bfhi(qb[j]);
  }
  na = half_sum(na) + bflo(qa2) * bflo(qa2) + bfhi(qa2) * bfhi(qa2);
  nb = half_sum(nb) + bflo(qb2) * bflo(qb2) + bfhi(qb2) * bfhi(qb2);
  PIN(na); PIN(nb);
  lds_wait(lds0 + SY_MF, 4);
  if (w == 0) __builtin_amdgcn_s_sleep(RSLEEP);
  kv_load(R, kvg + ((ROT + 1) & 7) * KV_HS, va + 2048);
  asm volatile("s_waitcnt vmcnt(10)" : "+v"(kx) :: "memory");
  unsigned ku = max(kx[0], kx[1]);
  ku = max(ku, (unsigned)__builtin_amdgcn_update_dpp(0, (int)ku, 0xB1, 0xF, 0xF, true));
  ku = max(ku, (unsigned)__builtin_amdgcn_update_dpp(0, (int)ku, 0x4E, 0xF, 0xF, true));
  ku = max(ku, (unsigned)__builtin_amdgcn_update_dpp(0, (int)ku, 0x141, 0xF, 0xF, true));
  ku = max(ku, (unsigned)__builtin_amdgcn_update_dpp(0, (int)ku, 0x140, 0xF, 0xF, true));
  const float km = __uint_as_float(max(max((unsigned)__builtin_amdgcn_readlane(ku, 0), (unsigned)__builtin_amdgcn_readlane(ku, 16)),
                                       max((unsigned)__builtin_amdgcn_readlane(ku, 32), (unsigned)__builtin_amdgcn_readlane(ku, 48))));
  float ma = sqrtf(na * km) * 1.001f, mb = sqrtf(nb * km) * 1.001f;
  const bf16x8 qa1 = __builtin_bit_cast(bf16x8, qa), qb1 = __builtin_bit_cast(bf16x8, qb);
  const int xcd = u & 7, j3 = u >> 3, sp = xcd >> 1, tile = ((xcd & 1) * 8 + (j3 >> 2)) * 4 + (j3 & 3);
  char* box = ws + WS_P + (size_t)(tile * 4 + sp) * 28672 + w * 7168 + 3072;
  f32x16 oa, ob;
  float la, lb;
  int tgt = 0;
  PIN(ma); PIN(mb);
  asm volatile("s_waitcnt vmcnt(5)" : "+v"(P[0]), "+v"(P[1]), "+v"(P[2]), "+v"(P[3]), "+v"(P[4]) :: "memory");
  kv_put((lds_ptr)smem + va, P);
  for (int attempt = 0;; ++attempt) {
#pragma unroll
    for (int i = 0; i < 16; ++i) { oa[i] = 0.f; ob[i] = 0.f; }
    attn_main(ws, smem, wo, w2, attempt == 0 && w < 3 && u < 216 ? u * 3 + w : -1, u, box, tag, tgt, R, w, lane, qa1, q_tail(qa2, ma, h), qb1, q_tail(qb2, mb, h), oa, ob);
    la = __shfl(oa[10], r); lb = __shfl(ob[10], r);
    if (attempt == 0 && !(la >= 8.2718061e-25f && lb >= 8.2718061e-25f)) *(lds_word)(uintptr_t)(lds0 + SY_BAD) = 1;
    if (w == 0) DRAIN();
    h_first(hf, ws, u, w, lane, 1);
    role_barrier(lds0 + SY_A, tgt);
    if (attempt || !__builtin_amdgcn_readfirstlane(lds_peek(lds0 + SY_BAD))) break;
    DRAIN_H(hf);
    ma = -INFINITY; mb = -INFINITY;
    attn_max(smem, kvg, tgt, w, lane, qa1, q_tail(qa2, 0.f, h), qb1, q_tail(qb2, 0.f, h), ma, mb);
    kv_load(P, kvg + (ROT & 7) * KV_HS, va + 2048);
    kv_load(R, kvg + ((ROT + 1) & 7) * KV_HS, va + 2048);
    KV_WAIT(P);
    KV_WAIT(R);
    kv_put((lds_ptr)smem + va, P);
  }
  __builtin_amdgcn_s_setprio(0);
  const char* wsrc = ws + WS_WP + (size_t)((j3 & 3) * 81 + sp * 20) * G2_STAGE + w * 9216;
#pragma unroll
  for (int i = 0; i < 9; ++i) glds16(wsrc + i * 1024, lane * 16, lds0 + w * 9216 + i * 1024);
  char* hp = ws + WS_H + (size_t)tb * NBLK * 1024;
  const u32x2 tails = {attn_store(hp, hd, lane, oa, la), attn_store(hp, hd + 1, lane, ob, lb)};
  if (lane < 32) store8_wt(hp + (320 + (hd >> 3)) * 1024 + (((hd >> 1) & 1) * 32 + lane) * 16 + ((hd >> 2) & 1) * 8, tails);
  fl = u32x2{0u, 0u};
  if (w == 0) asm volatile("global_load_dwordx2 %0, %1, off sc1" : "+v"(fl) : "v"(ws + WS_IN1 + u * 1024 + (IN_H + min(lane, 35)) * 8) : "memory");
  DRAIN_H(hf);
  PIN(fl);
  if (lds_arrive_rtn(lds0 + SY_AE) == 3 && lane < 8) {
    const int rd = lane < 4 ? 6 + (tb >> 5) + 8 * (((tb >> 2) & 7) * 4 + lane) : 2 * (lane - 4) + (xcd & 1) + 8 * j3;
    store8_wt(ws + WS_IN1 + rd * 1024 + (lane < 4 ? IN_O + (u & 15) : IN_B + sp) * 8, tag);
  }
#pragma unroll
  for (int i = 0; i < 9; ++i) glds16(wsrc + 2 * G2_STAGE + i * 1024, lane * 16, lds0 + 2 * G2_STAGE + w * 9216 + i * 1024);
}

DI float silu_mul(float g, float u) {
  const float e = __builtin_amdgcn_exp2f(g * -1.4426950408889634f);
  return g * __builtin_amdgcn_rcpf(1.f + e) * u;
}

constexpr int G1_W3 = 9216, G1_SLOT = G1_W3 + 9216, G1_RING = SY - 4 * G1_SLOT;
DI void glds16h(const void* sbase, unsigned voff, unsigned lds_addr) {
  asm volatile("s_mov_b32 m0, %0\n\ts_mov_b32 exec_hi, 0\n\tglobal_load_lds_dwordx4 %1, %2\n\ts_mov_b32 exec_hi, -1" ::"s"(lds_addr), "v"(voff), "s"(sbase) : "memory");
}
template <int OFF>
DI void gload16(u32x4& a, const void* sbase, unsigned voff) {
  asm volatile("global_load_dwordx4 %0, %1, %2 offset:%3 sc1" : "+v"(a) : "v"(voff), "s"(sbase), "i"(OFF) : "memory");
}

#define HAT 2
#define GPRIO 1
DI u32x2 gemm1_unit(char* smem, char* ws, const unsigned short* w1, const unsigned short* w3, u32x2 e, u32x4 (&hf)[6][2], int g1, int tg, int u, int w, int lane) {
  const unsigned lds0 = (unsigned)(uintptr_t)smem, syg = lds0 + SY_G;
  const int j = u >> 3, ct = (u & 7) * 2 + (j & 1), rt = j >> 1;
  const int pe = 80 * (ct >> 2) + 9 * (ct & 3), ls = 9 * ct / 44, pl = 36 + 9 * ct + 36 * ls, jb = 44 * ls + 44 - 9 * ct;
  const char* wsrc = (const char*)((w >> 1) ? w3 : w1) + (w & 1) * 16 * 9216 + pe * 32;
  const unsigned wdst = lds0 + G1_RING + (w >> 1) * G1_W3 + (w & 1) * 4608;
  unsigned woff[5], jm = 0;
#pragma unroll
  for (int i = 0; i < 5; ++i) {
    const unsigned ch = i * 64 + (i < 4 ? lane : lane & 31), row = ch / 18u, cc = ch - row * 18u;
    woff[i] = (row ^ ((row >> 1) & 4u)) * 9216u + cc * 16u;
    jm |= ((int)cc >= 2 * jb) << i;
  }
  const char* xsrc = ws + WS_XN + (size_t)(rt * 4 + w) * 36864;
  const unsigned xoff = lane * 16;
  int sw = rt * 9 / 8, sx = sw;
  u32x4 xf[3][2];
#pragma unroll
  for (int i = 0; i < 3; ++i) { xf[i][0] = u32x4{0, 0, 0, 0}; xf[i][1] = u32x4{0, 0, 0, 0}; }
#define G1_WP(s, i) do { if ((i) < 4) glds16(wsrc + (size_t)sw * 294912, woff[i], wdst + (~(s) & 3) * G1_SLOT + (i) * 1024); \
    else { glds16h(wsrc + (size_t)sw * 294912, woff[4], wdst + (~(s) & 3) * G1_SLOT + 4096); sw = sw == 17 ? 0 : sw + 1; } } while (0)
#define G1_XP(set, i) do { if ((i) == 0) gload16<0>(xf[set][0], xsrc + sx * 2048, xoff); \
    else { gload16<1024>(xf[set][1], xsrc + sx * 2048, xoff); sx = sx == 17 ? 0 : sx + 1; } } while (0)
#pragma unroll
  for (int i = 0; i < 5; ++i) G1_WP(0, i);
  asm volatile("s_waitcnt vmcnt(5) lgkmcnt(0)\n\ts_barrier" : "+v"(e) :: "memory");
  const u32x2 tag = make_tag(e);
#pragma unroll
  for (int i = 0; i < 5; ++i) G1_WP(1, i);
  if (w == 0) {
    lds_wait(lds0 + SY_XN, 4);
    if (lane < 16) store8_wt(ws + WS_IN1 + ((lane >> 1) + 8 * ((tg & ~1) + (lane & 1))) * 1024 + ((tg & 1) * 8 + g1) * 8, tag);
    poll_words(ws + WS_IN1 + u * 1024 + (lane & 15) * 8, 0xFFFF, tag);
    lds_arrive(lds0 + SY_PX);
  } else {
    DRAIN();
    lds_wait(lds0 + SY_PX, 1);
  }
  G1_XP(0, 0); G1_XP(0, 1); G1_XP(1, 0); G1_XP(1, 1);
  lds_wait(lds0 + SY_MF, 4);
#pragma unroll
  for (int i = 0; i < 5; ++i) G1_WP(2, i);
  __builtin_amdgcn_s_setprio(GPRIO);
  const int c = lane & 15, q = lane >> 4;
  const int aoff0 = G1_RING + (8 * q + 4 * (q & 1) + (c >> 2)) * 288 + 8 * (c & 3), aoff1 = G1_RING + (8 * q + 4 * ((q & 1) ^ 1) + (c >> 2)) * 288 + 8 * (c & 3);
#define G1_RD(p, i) trread((p) + ((i) / 9) * G1_W3 + ((i) % 9) * 32)
  bf16x4 al[6], ah[6];
  lds_arrive(syg);
  lds_wait(syg, 4);
#pragma unroll
  for (int i = 0; i < 6; ++i) { al[i] = G1_RD((lds_cptr)smem + 3 * G1_SLOT + aoff0, i); if (i < 5) ah[i] = G1_RD((lds_cptr)smem + 3 * G1_SLOT + aoff1, i); }
  int g = 0, seen = 0;
  f32x4 acc[36];
#define G1_STAGE(s, VM, LW, LX, HE) do { \
    asm volatile("s_waitcnt vmcnt(" #VM ")" : "+v"(xf[s][0]), "+v"(xf[s][1]) :: "memory"); \
    lds_arrive(syg); \
    const lds_cptr pc = (lds_cptr)smem + (~g & 3) * G1_SLOT, pn = (lds_cptr)smem + (~(g + 1) & 3) * G1_SLOT; \
    const lds_cptr pc0 = pc + aoff0, pc1 = pc + aoff1, pn0 = pn + aoff0, pn1 = pn + aoff1; \
    SBAR(); \
    _Pragma("unroll") for (int i = 0; i < 18; ++i) { \
      MFMA16A(acc[2 * i], cat(al[i % 6], ah[i % 6]), xf[s][0]); \
      SBAR(); \
      if (i == 9) LDS_CHECK(seen, syg, 4 * g + 8); \
      if (HE && i == 9 && g == 19) {        \
        DRAIN(); \
        if (lds_arrive_rtn(lds0 + SY_HE) == 3 && lane < 4) store8_wt(rdr + (2 * (ct >> 2)) * 1024 + (IN_H + (ct & 3)) * 8, tag); \
      } \
      ah[(i + 5) % 6] = i < 13 ? G1_RD(pc1, i + 5) : G1_RD(pn1, i - 13); \
      if (i == 4) seen = lds_peek(syg); \
      if (LW && (i & 1) && i >= 9) G1_WP(g + 3, (i - 9) >> 1); \
      if (LX && i == 1) G1_XP((s + 2) % 3, 0); \
      if (LX && i == 3) G1_XP((s + 2) % 3, 1); \
      SBAR(); \
      MFMA16A(acc[2 * i + 1], cat(al[i % 6], ah[i % 6]), xf[s][1]); \
      SBAR(); \
      al[i % 6] = i < 12 ? G1_RD(pc0, i + 6) : G1_RD(pn0, i - 12); \
      SBAR(); \
    } \
    ++g; } while (0)
  int known = 0;
  char* const rdr = ws + WS_IN1 + ((rt >> 3) + 8 * ((rt & 7) * 4 + (lane & 3))) * 1024;
  for (int un = 0; un < 2; ++un) {
#pragma unroll
    for (int b = 0; b < 36; ++b) acc[b] = f32x4{0.f, 0.f, 0.f, 0.f};
    for (int trip = un; trip < 6; ++trip) {
      if (g == 15) {
        wsrc += (pl - pe) * 32;
#pragma unroll
        for (int i = 0; i < 5; ++i) woff[i] += (jm >> i & 1) * 1152u;
      }
      G1_STAGE(0, 7, true, true, false); G1_STAGE(1, 7, true, true, true); G1_STAGE(2, 7, true, true, false);
    }
    if (un) { G1_STAGE(0, 7, false, true, false); G1_STAGE(1, 2, false, false, false); G1_STAGE(2, 0, false, false, false); }
    MFMA_DRAIN();
    known = un ? __builtin_amdgcn_readfirstlane(lds_peek(lds0 + SY_PH)) : 0;
    char* hp = ws + WS_H + ((size_t)(rt * 4 + w) * NBLK + (un ? pl : pe)) * 1024 + ((q & 1) * 32 + (q >> 1) * 16 + c) * 16;
#pragma unroll
    for (int b = 0; b < 9; ++b) {
      PIN(acc[2 * b]); PIN(acc[2 * b + 1]); PIN(acc[18 + 2 * b]); PIN(acc[19 + 2 * b]);
      unsigned t[2][2];
#pragma unroll
      for (int i = 0; i < 4; ++i) {
        const f32x4 &ga = acc[2 * b + (i >> 1)], &ua = acc[18 + 2 * b + (i >> 1)];
        t[i >> 1][i & 1] = cvtpk(silu_mul(ga[2 * (i & 1)], ua[2 * (i & 1)]), silu_mul(ga[2 * (i & 1) + 1], ua[2 * (i & 1) + 1]));
      }
      const auto s0 = __builtin_amdgcn_permlane32_swap(t[0][0], t[1][0], false, false), s1 = __builtin_amdgcn_permlane32_swap(t[0][1], t[1][1], false, false);
      store16_wt(hp + (b + (b >= 6 && un && b >= jb ? 36 : 0)) * 1024, (u32x4{s0[0], s1[0], s0[1], s1[1]}));
      if (b == HAT) h_first(hf, ws, u, w + 4, lane, known);
    }
  }
  __builtin_amdgcn_s_setprio(0);
  DRAIN_H(hf);
  if (lds_arrive_rtn(lds0 + SY_GE) == 3 && lane < 8) {
    const int sp = (9 * ct + 2 * (lane & 4)) / 44;
    store8_wt(rdr + 2 * sp * 1024 + (IN_H + 4 + ct - sp * 44 / 9) * 8, tag);
  }
  if (!known) {
    lds_wait(lds0 + SY_PH, 1);
    h_again(hf, ws, u, w + 4, lane, 1);
    DRAIN_H(hf);
  }
#undef G1_WP
#undef G1_XP
#undef G1_RD
#undef G1_STAGE
  return tag;
}

#define PSLEEP 1
template <int OFF>
DI void blds16(__amdgpu_buffer_rsrc_t r, unsigned voff, unsigned soff) {
  asm volatile("buffer_load_dwordx4 %0, %1, %2 offen offset:%3 lds" ::"v"(voff), "s"(r), "s"(soff), "i"(OFF) : "memory");
}
template <int OFF>
DI void blds4(__amdgpu_buffer_rsrc_t r, unsigned voff, unsigned soff) {
  asm volatile("buffer_load_dword %0, %1, %2 offen offset:%3 lds" ::"v"(voff), "s"(r), "s"(soff), "i"(OFF) : "memory");
}
template <int ADD>
DI void set_m0(unsigned base) { asm volatile("s_add_u32 m0, %0, %1" ::"s"(base), "i"(ADD) : "memory", "scc"); }
template <int N>
DI void wait_bar(u32x4& a, u32x4& b) { asm volatile("s_waitcnt vmcnt(%2)\n\ts_barrier" : "+v"(a), "+v"(b) : "i"(N) : "memory"); }

template <int T>
DI void g2_iter(f32x4 (&acc)[2][9], bf16x8 (&af)[9], u32x4 (&hf)[6][2], const lds_cptr (&ap)[3], __amdgpu_buffer_rsrc_t rw, __amdgpu_buffer_rsrc_t rh,
                unsigned w16, unsigned w4, unsigned hoff, unsigned hoff1, unsigned wdst, unsigned h20, const char* inh, u32x2 tag, u32x2& fl, u64& miss, unsigned need) {
  constexpr int NH = T == 3 ? 3 : (T + 4 < 20 ? T + 4 : 20) - T, NW = T < 3 ? T + 2 : (T + 5 < 20 ? T + 5 : 20) - T - 1;
  wait_bar<T == 1 ? 11 : 2 * NH + 3 * NW>(hf[T % 6][0], hf[T % 6][1]);
  if (T == 2 && __builtin_expect((unsigned)miss & need, 0)) asm volatile("global_load_dwordx2 %0, %1, off sc1" : "+v"(fl) : "v"(inh) : "memory");
  SBAR();
#pragma unroll
  for (int b = 0; b < 9; ++b) {
    MFMA16A(acc[0][b], af[b], hf[T % 6][0]);
    SBAR();
    if (T + 5 <= 20 && b == 0) bload16(hf[(T + 5) % 6][0], rh, hoff, T == 15 ? h20 : (T + 5) * 4096);
    if (T + 5 <= 20 && b == 1) bload16(hf[(T + 5) % 6][1], rh, hoff1, T == 15 ? h20 : (T + 5) * 4096);
    if (T + 6 <= 20 && b == 2) set_m0<((T + 6) & 7) * G2_STAGE>(wdst);
    if (T + 6 <= 20 && b == 3) blds16<0>(rw, w16, (T + 6) * G2_STAGE);
    if (T + 6 <= 20 && b == 5) blds16<1024>(rw, w16, (T + 6) * G2_STAGE);
    if (T + 6 <= 20 && b == 7) blds4<2048>(rw, w4, (T + 6) * G2_STAGE);
    SBAR();
    MFMA16A(acc[1][b], af[b], hf[T % 6][1]);
    SBAR();
    if (T < 20) af[b] = LDS128(ap[((T + 1) & 7) / 3] + (((T + 1) & 7) % 3) * G2_STAGE + b * 1024);
    SBAR();
  }
  if (T == 3 && __builtin_expect((unsigned)miss & need, 0)) {
    asm volatile("s_waitcnt vmcnt(10)" : "+v"(fl) :: "memory");
    miss &= ~__ballot(fl[0] == tag[0] && fl[1] == tag[1]);
    if ((unsigned)miss & need) miss &= ~poll_words(inh, need, tag);
  }
}
DI void g2_last(f32x4 (&acc)[2][9], bf16x8 (&af)[9], u32x4& h0, u32x4& h1, u32x4& d0, u32x4& d1, lds_ptr red) {
  asm volatile("s_waitcnt vmcnt(0)\n\ts_barrier" : "+v"(h0), "+v"(h1), "+v"(d0), "+v"(d1) :: "memory");
#pragma unroll
  for (int b = 0; b < 9; ++b) { MFMA16A(acc[1][b], af[b], h1); SBAR(); }
#pragma unroll
  for (int b = 0; b < 9; ++b) {
    MFMA16A(acc[0][b], af[b], h0);
    SBAR();
    *(__attribute__((address_space(3))) f32x4*)(red + b * 1024) = acc[1][b];
    SBAR();
  }
}
template <int T, int E, typename... A>
DI void g2_run(A&... a) { if constexpr (T < E) { g2_iter<T>(a...); g2_run<T + 1, E>(a...); } }

DI void gemm2_unit(char* smem, const unsigned short* x, char* ws, unsigned short* out, u32x2 tag, u32x4 (&hf)[6][2], u32x2 fl, int u, int w, int lane) {
  const unsigned lds0 = (unsigned)(uintptr_t)smem;
  const int xcd = u & 7, j = u >> 3;
  const int sp = xcd >> 1, rt = (xcd & 1) * 8 + (j >> 2), ct = j & 3;
  const int wm = w & 3, kg = w >> 2;
  const __amdgpu_buffer_rsrc_t rw = __builtin_amdgcn_make_buffer_rsrc(ws + WS_WP + (unsigned)__builtin_amdgcn_readfirstlane((ct * 81 + sp * 20) * G2_STAGE + w * 2304), 0, 21 * G2_STAGE, 0x00020000);
  const __amdgpu_buffer_rsrc_t rh = h_rsrc(ws, u, w);
  unsigned w16 = lane * 16, w4 = lane * 4, wdst = lds0 + w * 2304, h20 = sp == 3 ? 20 * 4096 : 19 * 4096;
  unsigned hoff = h_off(w, lane), hoff1 = hoff ^ 256;
  const char* inh = ws + WS_IN1 + u * 1024 + (IN_H + min(lane, 35)) * 8;
  unsigned need = sp == 3 ? 0xFFFF0030u : sp ? 0x3F0u : 0x1F0u;
  u64 miss = w == 0 ? ~__ballot(fl[0] == tag[0] && fl[1] == tag[1]) : 0ull;
  const unsigned orow = (rt * 128 + wm * 32 + kg * 16) * D_ + ct * 144;
  const unsigned oi = orow + (lane >> 2) * D_ + sp * 32 + (lane & 3) * 8, ot = orow + ((lane & 31) >> 1) * D_ + 128 + (lane & 1) * 8;
  u32x4 xv, xt;
#pragma unroll
  for (int s = 3; s < 5; ++s) { bload16(hf[s][0], rh, hoff, s * 4096); bload16(hf[s][1], rh, hoff1, s * 4096); }
  asm volatile("s_barrier" ::: "memory");
#define G2_PW(s) do { set_m0<(s) * G2_STAGE>(wdst); asm volatile("s_nop 0"); blds16<0>(rw, w16, (s) * G2_STAGE); blds16<1024>(rw, w16, (s) * G2_STAGE); blds4<2048>(rw, w4, (s) * G2_STAGE); } while (0)
  G2_PW(4); G2_PW(5);
#undef G2_PW
  f32x4 acc[2][9];
#pragma unroll
  for (int i = 0; i < 2; ++i)
#pragma unroll
    for (int b = 0; b < 9; ++b) { acc[i][b] = f32x4{0.f, 0.f, 0.f, 0.f}; PIN(acc[i][b]); }
  lds_cptr ap[3];
#pragma unroll
  for (int i = 0; i < 3; ++i) { ap[i] = (lds_cptr)smem + i * 3 * G2_STAGE + kg * 9216 + lane * 16; PIN(ap[i]); }
  bf16x8 af[9];
#pragma unroll
  for (int b = 0; b < 9; ++b) af[b] = LDS128(ap[0] + b * 1024);
  g2_run<0, 16>(acc, af, hf, ap, rw, rh, w16, w4, hoff, hoff1, wdst, h20, inh, tag, fl, miss, need);
  asm volatile("global_load_dwordx4 %0, %2, %4\n\tglobal_load_dwordx4 %1, %3, %4" : "=&v"(xv), "=&v"(xt) : "v"(oi * 2), "v"(ot * 2), "s"(x) : "memory");
  g2_run<16, 19>(acc, af, hf, ap, rw, rh, w16, w4, hoff, hoff1, wdst, h20, inh, tag, fl, miss, need);
  const int ro = (wm == 3 ? 5 : wm) * G2_STAGE + lane * 16;
  const f32x4* red = (const f32x4*)(smem + ro);
  if (sp == 3) g2_iter<19>(acc, af, hf, ap, rw, rh, w16, w4, hoff, hoff1, wdst, h20, inh, tag, fl, miss, need);
  g2_last(acc, af, hf[20 % 6][0], hf[20 % 6][1], hf[19 % 6][0], hf[19 % 6][1], (lds_ptr)smem + ro + (kg ^ 1) * 9216);
  char* box = ws + WS_P + (size_t)(rt * 4 + ct) * (4 * 28672) + lane * 16;
  const char* in = box + sp * 28672 + w * 1024;
  const char* tin = box + sp * 28672 + 8192 + kg * 512;
  u32x4 r[6];
  u64 m[6];
#pragma unroll
  for (int i = 0; i < 6; ++i) { r[i] = u32x4{0u, 0u, 0u, 0u}; m[i] = i < 3 ? ~0ull : wm == sp ? 0xFFFFFFFFull : 0ull; }
#define FETCH(WAIT) asm volatile("s_mov_b64 exec, %12\n\tglobal_load_dwordx4 %0, %6, off sc1\n\ts_mov_b64 exec, %13\n\tglobal_load_dwordx4 %1, %7, off sc1\n\t" \
                 "s_mov_b64 exec, %14\n\tglobal_load_dwordx4 %2, %8, off sc1\n\ts_mov_b64 exec, %15\n\tglobal_load_dwordx4 %3, %9, off sc1\n\t" \
                 "s_mov_b64 exec, %16\n\tglobal_load_dwordx4 %4, %10, off sc1\n\ts_mov_b64 exec, %17\n\tglobal_load_dwordx4 %5, %11, off sc1\n\t" \
                 "s_mov_b64 exec, -1\n\t" WAIT \
                 : "+v"(r[0]), "+v"(r[1]), "+v"(r[2]), "+v"(r[3]), "+v"(r[4]), "+v"(r[5]) \
                 : "v"(in), "v"(in + 9216), "v"(in + 18432), "v"(tin), "v"(tin + 9216), "v"(tin + 18432), \
                   "s"(m[0]), "s"(m[1]), "s"(m[2]), "s"(m[3]), "s"(m[4]), "s"(m[5]) : "memory")
  asm volatile("s_nop 7");
  if (miss >> 32 & 15) poll_words(inh, 0xFull << 32, tag);
  BARRIER_LGKM();
#pragma unroll
  for (int b = 0; b < 9; ++b) PIN(acc[0][b]);
  f32x4 v[10];
#pragma unroll
  for (int b = 0; b < 9; ++b) v[b] = acc[0][b] + red[kg * 576 + b * 64];
#pragma unroll
  for (int i = 0; i < 4; ++i) v[9][i] = __uint_as_float(__builtin_amdgcn_permlane32_swap(__float_as_uint(v[8][i]), __float_as_uint(v[8][i]), false, false)[1]);
#define PK8(a, b) u32x4{min(cvtpk(a[0], a[1]), ~1u), min(cvtpk(a[2], a[3]), ~1u), min(cvtpk(b[0], b[1]), ~1u), min(cvtpk(b[2], b[3]), ~1u)}
#pragma unroll
  for (int j = 0; j < 4; ++j)
    if (j != sp) store16_wt(box + j * 28672 + ((sp - j - 1) & 3) * 9216 + w * 1024, PK8(v[2 * j], v[2 * j + 1]));
  if (wm != sp && lane < 32) store16_wt(box + wm * 28672 + ((sp - wm - 1) & 3) * 9216 + 8192 + kg * 512, PK8(v[8], v[9]));
#undef PK8
  f32x4 lo = v[0], hi = v[1];
#pragma unroll
  for (int j = 1; j < 4; ++j)
    if (j == sp) { lo = v[2 * j]; hi = v[2 * j + 1]; }
  FETCH("s_waitcnt vmcnt(0)");
#pragma nounroll
  for (int spin = 0; spin < GSPIN; ++spin) {
#pragma unroll
    for (int i = 0; i < 6; ++i) m[i] = __ballot(max(max(r[i][0], r[i][1]), max(r[i][2], r[i][3])) == ~0u);
    if (!(m[0] | m[1] | m[2] | m[3] | m[4] | m[5])) break;
    __builtin_amdgcn_s_sleep(PSLEEP);
    FETCH("s_waitcnt vmcnt(0)");
  }
#undef FETCH
#define FIN(o, xx, a, b, r0, src) _Pragma("unroll") for (int k = 0; k < 4; ++k) { \
    const float e = (k < 2 ? a[2 * k] : b[2 * k - 4]) + bflo(r[r0][k]) + (bflo(r[r0 + 1][k]) + bflo(r[r0 + 2][k])); \
    const float f = (k < 2 ? a[2 * k + 1] : b[2 * k - 3]) + bfhi(r[r0][k]) + (bfhi(r[r0 + 1][k]) + bfhi(r[r0 + 2][k])); \
    o[k] = cvtpk(bflo(xx[k]) + __shfl(e, src), bfhi(xx[k]) + __shfl(f, src)); }
  u32x4 o;
  PIN(xv); PIN(xt);
  FIN(o, xv, lo, hi, 0, (lane & 3) * 16 + (lane >> 2));
  store16_wt(out + oi, o);
  if (wm == sp) {
    FIN(o, xt, v[8], v[9], 3, (lane & 1) * 16 + ((lane & 31) >> 1));
    if (lane < 32) store16_wt(out + ot, o);
  }
#undef FIN
}

__global__ __launch_bounds__(512, 2) void fused_block(const unsigned short* __restrict__ x, const unsigned short* __restrict__ wq, const unsigned short* __restrict__ wk,
                                                      const unsigned short* __restrict__ wv, const unsigned short* __restrict__ wo, const unsigned short* __restrict__ w1,
                                                      const unsigned short* __restrict__ w3, const unsigned short* __restrict__ w2, char* __restrict__ ws,
                                                      unsigned short* __restrict__ out) {
  extern __shared__ __attribute__((aligned(16))) char smem[];
  const int lane = threadIdx.x & 63, u = blockIdx.x;
  const int w = __builtin_amdgcn_readfirstlane(threadIdx.x >> 6);
  const int g = 7 - (u >> 5), tg = (u & 7) * 4 + ((u >> 3) & 3);
  const char* pc;
  unsigned len;
  asm volatile("s_getpc_b64 %0\n.Lfb_head:\n\ts_mov_b32 %1, .Lfb_tail-.Lfb_head-16" : "=s"(pc), "=s"(len));
  if (w >= 6) {
    const unsigned j = u >> 3, o = w == 7 ? j * 2048 : (j & 7) * 1024, o2 = w == 7 ? o + 1024 : o + CODE2;
    glds16(pc, min(o + lane * 16, len), (unsigned)(uintptr_t)smem + K1_XT + 17408 + (w & 1) * 2048);
    glds16(pc, min(o2 + lane * 16, len), (unsigned)(uintptr_t)smem + K1_XT + 18432 + (w & 1) * 2048);
  }
  u32x2 e, tag, fl = {0u, 0u};
  u32x4 hf[6][2];
  asm volatile("global_load_dwordx2 %0, %1, %2 sc1" : "=v"(e) : "v"(0), "s"(ws + WS_E) : "memory");
  if (w >= 4) {
    if (w == 4 && lane < 16) ((lds_word)(smem + SY))[lane] = 0;
    p1_load(smem, wq, wk, wv, g, tg, w, lane);
    tag = gemm1_unit(smem, ws, w1, w3, e, hf, g, tg, u, w - 4, lane);
  } else {
    __builtin_amdgcn_s_setprio(3);
    tag = p1_compute(smem, x, ws, g, tg, w, lane, e);
    attn_unit(smem, ws, wo, w2, tag, hf, fl, u, w, lane);
  }
  gemm2_unit(smem, x, ws, out, tag, hf, fl, u, w, lane);
  asm volatile(".Lfb_tail:");
  if (u == 0 && w == 0 && lane == 0) store8_wt(ws + WS_E, tag);
}

extern "C" void kernel_launch(void* const* d_in, const int* in_sizes, int n_in, void* d_out, int out_size, void* d_ws, size_t ws_size,
                              hipStream_t stream) {
  (void)hipFuncSetAttribute((const void*)fused_block, hipFuncAttributeMaxDynamicSharedMemorySize, LDS_ALL);
  fused_block<<<256, 512, LDS_ALL, stream>>>((const unsigned short*)d_in[0], (const unsigned short*)d_in[1], (const unsigned short*)d_in[2],
                                             (const unsigned short*)d_in[3], (const unsigned short*)d_in[4], (const unsigned short*)d_in[6],
                                             (const unsigned short*)d_in[7], (const unsigned short*)d_in[8], (char*)d_ws, (unsigned short*)d_out);
}
```
